```python
import torch

def _softmax(x, axis=-1):
    return torch.softmax(x, dim=axis)
def _gelu(x):
    return torch.nn.functional.gelu(x, approximate="tanh")
def _silu(x):
    return torch.nn.functional.silu(x)
def _sigmoid(x):
    return torch.sigmoid(x)
def _layernorm(x, axis=-1, eps=1e-5):
    m = x.mean(dim=axis, keepdim=True)
    v = x.var(dim=axis, keepdim=True, unbiased=False)
    return (x - m) / torch.sqrt(v + eps)
def _scatter_add(tgt, idx, src, axis=0):
    return tgt.clone().index_add_(axis, idx, src)
def _segment_sum(src, seg, n):
    out = torch.zeros((n,) + src.shape[1:], dtype=src.dtype)
    return out.index_add_(0, seg, src)
def _pad(x, pw):
    flat = tuple(v for pair in reversed(pw) for v in pair)
    return torch.nn.functional.pad(x, flat)

kernel_name = 'concat_heads__linear_attn_block__ssm_conv_s5__prompt'

def setup_inputs():

    _w = (lambda a: (lambda t: t.to(torch.bfloat16) if t.is_floating_point() else t)(torch.as_tensor(a)))
    def _A_setup():
        B, S, D = 1, 2048, 512
        return {
            "x": _w((lambda *s: torch.randn(*s, dtype=torch.bfloat16) * 0.05)(B, S, D)),
            "wqkv": _w((lambda *s: torch.randn(*s, dtype=torch.bfloat16) * 0.4)(D, 3 * D)),
            "wo": _w((lambda *s: torch.randn(*s, dtype=torch.bfloat16) * 0.05)(D, D)),
        }
    _A_in = _A_setup()

    def _B_setup():
        B, S, D = 1, 2048, 512
        P = D
        Lambda_re = -0.5 * (lambda _x: torch.exp(torch.as_tensor(_x)))((lambda *s: torch.randn(*s, dtype=torch.bfloat16) * 0.2)(P))
        Lambda_im = (lambda *s: torch.randn(*s, dtype=torch.bfloat16) * 0.8)(P) * 1.0
        B_re = (lambda *s: torch.randn(*s, dtype=torch.bfloat16) * 0.2)(P, D) * (D ** -0.5)
        B_im = (lambda *s: torch.randn(*s, dtype=torch.bfloat16) * 0.2)(P, D) * (D ** -0.5)
        C_re = (lambda *s: torch.randn(*s, dtype=torch.bfloat16) * 0.2)(D, P) * (P ** -0.5)
        C_im = (lambda *s: torch.randn(*s, dtype=torch.bfloat16) * 0.2)(D, P) * (P ** -0.5)
        log_dt = (lambda *s: torch.randn(*s, dtype=torch.bfloat16) * 0.2)(P) * 0.3 - 2.0
        Dskip = (lambda *s: torch.randn(*s, dtype=torch.bfloat16) * 0.05)(D)
        return {
            "x": _w((lambda *s: torch.randn(*s, dtype=torch.bfloat16) * 0.05)(B, S, D)),
            "Lambda_re": _w(Lambda_re),
            "Lambda_im": _w(Lambda_im),
            "B_re": _w(B_re),
            "B_im": _w(B_im),
            "C_re": _w(C_re),
            "C_im": _w(C_im),
            "log_dt": _w(log_dt),
            "Dskip": _w(Dskip),
        }
    _B_in = _B_setup()

    return {
        "A_x": _A_in["x"],
        "A_wqkv": _A_in["wqkv"],
        "A_wo": _A_in["wo"],
        "B_x": _B_in["x"],
        "B_Lambda_re": _B_in["Lambda_re"],
        "B_Lambda_im": _B_in["Lambda_im"],
        "B_B_re": _B_in["B_re"],
        "B_B_im": _B_in["B_im"],
        "B_C_re": _B_in["C_re"],
        "B_C_im": _B_in["C_im"],
        "B_log_dt": _B_in["log_dt"],
        "B_Dskip": _B_in["Dskip"],
    }

def reference(A_x, A_wqkv, A_wo, B_x, B_Lambda_re, B_Lambda_im, B_B_re, B_B_im, B_C_re, B_C_im, B_log_dt, B_Dskip):
    def _A_ref(x, wqkv, wo):
        B, S, D = x.shape
        H = 32; Dh = D // H
        qkv = (lambda x, *d: x.permute(*d))((x @ wqkv).reshape(B, S, 3, H, Dh), 2, 0, 3, 1, 4)
        q, k, v = qkv[0], qkv[1], qkv[2]
        phi = lambda t: torch.where(t > 0, t + 1, torch.exp(t)) + 1e-6
        pq, pk = phi(q), phi(k)
        kv = torch.einsum("bhsd,bhse->bhde", pk, v)
        z = torch.einsum("bhsd->bhd", pk)
        num = torch.einsum("bhqd,bhde->bhqe", pq, kv)
        den = torch.einsum("bhqd,bhd->bhq", pq, z)[..., None] + 1e-6
        o = num / den
        return (lambda x, *d: x.permute(*d))(o, 0, 2, 1, 3).reshape(B, S, D) @ wo

    def _B_ref(x, Lambda_re, Lambda_im, B_re, B_im, C_re, C_im, log_dt, Dskip):
        Bsz, L, H = x.shape
        Lam = Lambda_re + 1j * Lambda_im
        Bmat = B_re + 1j * B_im
        Cmat = C_re + 1j * C_im
        dt = torch.exp(log_dt)
        Lam_bar = torch.exp(dt * Lam)
        B_bar = ((Lam_bar - 1.0) / Lam)[:, None] * Bmat
        Bu = torch.einsum("bld,pd->blp", x + 0j, B_bar)
        h = torch.zeros_like(Bu[:, 0, :])
        hs = []
        for t in range(L):
            h = Lam_bar[None, :] * h + Bu[:, t, :]
            hs.append(h)
        hseq = torch.stack(hs, 1)
        y = 2.0 * torch.einsum("blp,dp->bld", hseq, Cmat).real + x * Dskip[None, None, :]
        return y

    a = _A_ref(A_x, A_wqkv, A_wo)
    b = _B_ref(B_x, B_Lambda_re, B_Lambda_im, B_B_re, B_B_im, B_C_re, B_C_im, B_log_dt, B_Dskip)
    return torch.concatenate([a, b], axis=-1)
```

```cpp
#include <hip/hip_runtime.h>
#include <hip/hip_bf16.h>
#include <stdint.h>
namespace naive {
typedef unsigned short u16;
__device__ __forceinline__ float bf2f(u16 v) { return __uint_as_float(((unsigned)v) << 16); }
__device__ __forceinline__ float rbf(float x) { unsigned u = __float_as_uint(x); u += 0x7fffu + ((u >> 16) & 1u); return __uint_as_float(u & 0xffff0000u); }
constexpr int S = 2048, D = 512, P = 512, H = 32, DH = 16;
__global__ void k_qkv(const u16* x, const u16* w, float* qkv) {
  int n = blockIdx.x * 256 + threadIdx.x, s = blockIdx.y;
  float a = 0; for (int k = 0; k < D; k++) a += bf2f(x[s * D + k]) * bf2f(w[k * 3 * D + n]);
  qkv[s * 3 * D + n] = rbf(a);
}
__device__ __forceinline__ float phi(float t) { float r = t > 0 ? rbf(t + 1.f) : rbf(expf(t)); return rbf(r + 1e-6f); }
__global__ void k_kv(const float* qkv, float* kv, float* z) {
  int h = blockIdx.x, d = threadIdx.x >> 4, e = threadIdx.x & 15;
  float a = 0, zz = 0;
  for (int s = 0; s < S; s++) { float pk = phi(qkv[s * 3 * D + D + h * DH + d]); a += pk * qkv[s * 3 * D + 2 * D + h * DH + e]; zz += pk; }
  kv[(h * DH + d) * DH + e] = rbf(a); if (e == 0) z[h * DH + d] = rbf(zz);
}
__global__ void k_o(const float* qkv, const float* kv, const float* z, float* o) {
  int s = blockIdx.x, c = threadIdx.x, h = c >> 4, e = c & 15;
  float num = 0, den = 0;
  for (int d = 0; d < DH; d++) { float pq = phi(qkv[s * 3 * D + h * DH + d]); num += pq * kv[(h * DH + d) * DH + e]; den += pq * z[h * DH + d]; }
  o[s * D + c] = rbf(rbf(num) / rbf(rbf(den) + 1e-6f));
}
__global__ void k_outA(const float* o, const u16* wo, float* out) {
  int n = threadIdx.x, s = blockIdx.x;
  float a = 0; for (int k = 0; k < D; k++) a += o[s * D + k] * bf2f(wo[k * D + n]);
  out[s * 2 * D + n] = rbf(a);
}
__global__ void k_bu(const u16* x, const u16* lre, const u16* lim, const u16* bre, const u16* bim, const u16* ldt, float2* bu, float2* lam) {
  int p = threadIdx.x, s = blockIdx.x;
  double lr = bf2f(lre[p]), li = bf2f(lim[p]); double dt = rbf(expf(bf2f(ldt[p])));
  double m = exp(dt * lr), ar = m * cos(dt * li), ai = m * sin(dt * li);
  double nr = ar - 1.0, ni = ai, dd = lr * lr + li * li;
  double cr = (nr * lr + ni * li) / dd, ci = (ni * lr - nr * li) / dd;
  double ur = 0, ui = 0;
  for (int k = 0; k < D; k++) { double xv = bf2f(x[s * D + k]); ur += xv * bf2f(bre[p * D + k]); ui += xv * bf2f(bim[p * D + k]); }
  bu[s * P + p] = make_float2((float)(cr * ur - ci * ui), (float)(cr * ui + ci * ur));
  if (s == 0) lam[p] = make_float2((float)ar, (float)ai);
}
__global__ void k_scan(float2* bu, const float2* lam) {
  int p = blockIdx.x * 64 + threadIdx.x; float2 l = lam[p]; float hr = 0, hi = 0;
  for (int s = 0; s < S; s++) { float2 u = bu[s * P + p]; float nr = l.x * hr - l.y * hi + u.x, ni = l.x * hi + l.y * hr + u.y; hr = nr; hi = ni; bu[s * P + p] = make_float2(hr, hi); }
}
__global__ void k_y(const float2* h, const u16* cre, const u16* cim, const u16* x, const u16* dsk, float* out) {
  int d = threadIdx.x, s = blockIdx.x; double a = 0;
  for (int p = 0; p < P; p++) { float2 hv = h[s * P + p]; a += (double)hv.x * bf2f(cre[d * P + p]) - (double)hv.y * bf2f(cim[d * P + p]); }
  out[s * 2 * D + D + d] = (float)(2.0 * a) + rbf(bf2f(x[s * D + d]) * bf2f(dsk[d]));
}
inline void launch(void* const* in, void* d_out, void* d_ws, hipStream_t st) {
  float* ws = (float*)d_ws; float* qkv = ws; float* kv = qkv + S * 3 * D; float* z = kv + H * DH * DH; float* o = z + H * DH;
  float2* bu = (float2*)(o + S * D); float2* lam = bu + S * P;
  float* out = (float*)d_out;
  k_qkv<<<dim3(6, S), 256, 0, st>>>((const u16*)in[0], (const u16*)in[1], qkv);
  k_kv<<<H, 256, 0, st>>>(qkv, kv, z);
  k_o<<<S, D, 0, st>>>(qkv, kv, z, o);
  k_outA<<<S, D, 0, st>>>(o, (const u16*)in[2], out);
  k_bu<<<S, P, 0, st>>>((const u16*)in[3], (const u16*)in[4], (const u16*)in[5], (const u16*)in[6], (const u16*)in[7], (const u16*)in[10], bu, lam);
  k_scan<<<P / 64, 64, 0, st>>>(bu, lam);
  k_y<<<S, D, 0, st>>>(bu, (const u16*)in[8], (const u16*)in[9], (const u16*)in[3], (const u16*)in[11], out);
}
}
extern "C" void kernel_launch(void* const* d_in, const int* in_sizes, int n_in, void* d_out, int out_size, void* d_ws, size_t ws_size, hipStream_t stream) {
  naive::launch(d_in, d_out, d_ws, stream);
}
```

```cpp
#define NAIVE_NO_ENTRY
#include <hip/hip_runtime.h>
#include <hip/hip_bf16.h>
#include <stdint.h>
namespace naive {
typedef unsigned short u16;
__device__ __forceinline__ float bf2f(u16 v) { return __uint_as_float(((unsigned)v) << 16); }
__device__ __forceinline__ float rbf(float x) { unsigned u = __float_as_uint(x); u += 0x7fffu + ((u >> 16) & 1u); return __uint_as_float(u & 0xffff0000u); }
constexpr int S = 2048, D = 512, P = 512, H = 32, DH = 16;
__global__ void k_qkv(const u16* x, const u16* w, float* qkv) {
  int n = blockIdx.x * 256 + threadIdx.x, s = blockIdx.y;
  float a = 0; for (int k = 0; k < D; k++) a += bf2f(x[s * D + k]) * bf2f(w[k * 3 * D + n]);
  qkv[s * 3 * D + n] = rbf(a);
}
__device__ __forceinline__ float phi(float t) { float r = t > 0 ? rbf(t + 1.f) : rbf(expf(t)); return rbf(r + 1e-6f); }
__global__ void k_kv(const float* qkv, float* kv, float* z) {
  int h = blockIdx.x, d = threadIdx.x >> 4, e = threadIdx.x & 15;
  float a = 0, zz = 0;
  for (int s = 0; s < S; s++) { float pk = phi(qkv[s * 3 * D + D + h * DH + d]); a += pk * qkv[s * 3 * D + 2 * D + h * DH + e]; zz += pk; }
  kv[(h * DH + d) * DH + e] = rbf(a); if (e == 0) z[h * DH + d] = rbf(zz);
}
__global__ void k_o(const float* qkv, const float* kv, const float* z, float* o) {
  int s = blockIdx.x, c = threadIdx.x, h = c >> 4, e = c & 15;
  float num = 0, den = 0;
  for (int d = 0; d < DH; d++) { float pq = phi(qkv[s * 3 * D + h * DH + d]); num += pq * kv[(h * DH + d) * DH + e]; den += pq * z[h * DH + d]; }
  o[s * D + c] = rbf(rbf(num) / rbf(rbf(den) + 1e-6f));
}
__global__ void k_outA(const float* o, const u16* wo, float* out) {
  int n = threadIdx.x, s = blockIdx.x;
  float a = 0; for (int k = 0; k < D; k++) a += o[s * D + k] * bf2f(wo[k * D + n]);
  out[s * 2 * D + n] = rbf(a);
}
__global__ void k_bu(const u16* x, const u16* lre, const u16* lim, const u16* bre, const u16* bim, const u16* ldt, float2* bu, float2* lam) {
  int p = threadIdx.x, s = blockIdx.x;
  double lr = bf2f(lre[p]), li = bf2f(lim[p]); double dt = rbf(expf(bf2f(ldt[p])));
  double m = exp(dt * lr), ar = m * cos(dt * li), ai = m * sin(dt * li);
  double nr = ar - 1.0, ni = ai, dd = lr * lr + li * li;
  double cr = (nr * lr + ni * li) / dd, ci = (ni * lr - nr * li) / dd;
  double ur = 0, ui = 0;
  for (int k = 0; k < D; k++) { double xv = bf2f(x[s * D + k]); ur += xv * bf2f(bre[p * D + k]); ui += xv * bf2f(bim[p * D + k]); }
  bu[s * P + p] = make_float2((float)(cr * ur - ci * ui), (float)(cr * ui + ci * ur));
  if (s == 0) lam[p] = make_float2((float)ar, (float)ai);
}
__global__ void k_scan(float2* bu, const float2* lam) {
  int p = blockIdx.x * 64 + threadIdx.x; float2 l = lam[p]; float hr = 0, hi = 0;
  for (int s = 0; s < S; s++) { float2 u = bu[s * P + p]; float nr = l.x * hr - l.y * hi + u.x, ni = l.x * hi + l.y * hr + u.y; hr = nr; hi = ni; bu[s * P + p] = make_float2(hr, hi); }
}
__global__ void k_y(const float2* h, const u16* cre, const u16* cim, const u16* x, const u16* dsk, float* out) {
  int d = threadIdx.x, s = blockIdx.x; double a = 0;
  for (int p = 0; p < P; p++) { float2 hv = h[s * P + p]; a += (double)hv.x * bf2f(cre[d * P + p]) - (double)hv.y * bf2f(cim[d * P + p]); }
  out[s * 2 * D + D + d] = (float)(2.0 * a) + rbf(bf2f(x[s * D + d]) * bf2f(dsk[d]));
}
inline void launch(void* const* in, void* d_out, void* d_ws, hipStream_t st) {
  float* ws = (float*)d_ws; float* qkv = ws; float* kv = qkv + S * 3 * D; float* z = kv + H * DH * DH; float* o = z + H * DH;
  float2* bu = (float2*)(o + S * D); float2* lam = bu + S * P;
  float* out = (float*)d_out;
  k_qkv<<<dim3(6, S), 256, 0, st>>>((const u16*)in[0], (const u16*)in[1], qkv);
  k_kv<<<H, 256, 0, st>>>(qkv, kv, z);
  k_o<<<S, D, 0, st>>>(qkv, kv, z, o);
  k_outA<<<S, D, 0, st>>>(o, (const u16*)in[2], out);
  k_bu<<<S, P, 0, st>>>((const u16*)in[3], (const u16*)in[4], (const u16*)in[5], (const u16*)in[6], (const u16*)in[7], (const u16*)in[10], bu, lam);
  k_scan<<<P / 64, 64, 0, st>>>(bu, lam);
  k_y<<<S, D, 0, st>>>(bu, (const u16*)in[8], (const u16*)in[9], (const u16*)in[3], (const u16*)in[11], out);
}
}

typedef short bf16x8 __attribute__((ext_vector_type(8)));
typedef float f32x16 __attribute__((ext_vector_type(16)));
#define LAS __attribute__((address_space(3)))
constexpr int NSLOT = 3;
constexpr int ROT = 0, WHICH = -1;
__global__ __launch_bounds__(256, 1) __attribute__((amdgpu_waves_per_eu(1,1))) void probe(const char* xa, const char* xb_, const char* wa, const char* wb0, const char* wb1, float* sink) {
  extern __shared__ __attribute__((aligned(16))) char lds[];
  const int b = blockIdx.x, xcd = b & 7, j = b >> 3;
  const int role = xcd >> 2, tl = j >> 3, tt = (xcd & 3) * 4 + tl, g = j & 7;
  const int tid = threadIdx.x, lane = tid & 63, wv = __builtin_amdgcn_readfirstlane(tid >> 6);
  unsigned long long t0 = __builtin_amdgcn_s_memtime();
  const char* xb = (role ? xb_ : xa) + (size_t)tt * 131072;
  const char* wb = role ? (g < 4 ? wb0 + g * 131072 : wb1 + (g - 4) * 131072) : wa + (size_t)g * 131072;
  const int prow = lane >> 3, pc = lane & 7;
  unsigned xo[4];
  #pragma unroll
  for (int i = 0; i < 4; i++) { int row = (i * 4 + wv) * 8 + prow; xo[i] = row * 1024 + ((pc ^ ((row >> 1) & 7)) * 16); }
  int r0 = ROT ? (g + 2 * tl) : 0;
  auto issue = [&](int c, int i) {
    int kc = (c + r0) & 7; int slot = c % NSLOT;
    const char* src = ((i & 1) ? wb : xb) + kc * 128 + xo[i >> 1];
    __builtin_amdgcn_global_load_lds((const unsigned*)src, (LAS unsigned*)(lds + slot * 32768 + (i & 1) * 16384 + ((i >> 1) * 4 + wv) * 1024), 16, 0, 0);
  };
  constexpr int PF = NSLOT - 1;
  #pragma unroll
  for (int c = 0; c < PF; c++) {
    #pragma unroll
    for (int i = 0; i < 8; i++) issue(c, i);
  }
  const int wm = wv >> 1, wn = wv & 1, r = lane & 31, h = lane >> 5;
  unsigned aoff[2], boff[2];
  #pragma unroll
  for (int m = 0; m < 2; m++) { int ra = wm * 64 + m * 32 + r; aoff[m] = ra * 128; int rb = wn * 64 + m * 32 + r; boff[m] = 16384 + rb * 128; }
  const unsigned sw = (r >> 1) & 7;
  f32x16 acc[2][2] = {};
  unsigned long long tsel = 0;
  bf16x8 a[2][2], bb[2][2];
  auto rd = [&](int c, int s) {
    const char* base = lds + (c % NSLOT) * 32768;
    #pragma unroll
    for (int m = 0; m < 2; m++) { a[s & 1][m] = *(const bf16x8*)(base + aoff[m] + (((2 * s + h) ^ sw) * 16)); bb[s & 1][m] = *(const bf16x8*)(base + boff[m] + (((2 * s + h) ^ sw) * 16)); }
  };
  auto mm = [&](int s, int cis, int i0) {
    #pragma unroll
    for (int m = 0; m < 2; m++) {
      #pragma unroll
      for (int n = 0; n < 2; n++) acc[m][n] = __builtin_amdgcn_mfma_f32_32x32x16_bf16(a[s & 1][m], bb[s & 1][n], acc[m][n], 0, 0, 0);
      if (cis >= 0 && cis < 8) issue(cis, i0 + m);
    }
  };
  #pragma unroll
  for (int c = 0; c < 8; c++) {
    asm volatile("s_waitcnt lgkmcnt(0)" ::: "memory");
    if (c < 7) asm volatile("s_waitcnt vmcnt(8)" ::: "memory"); else asm volatile("s_waitcnt vmcnt(0)" ::: "memory");
    __builtin_amdgcn_s_barrier();
    __builtin_amdgcn_sched_barrier(0);
    if (c == WHICH) { tsel = __builtin_amdgcn_s_memtime(); asm volatile("s_waitcnt lgkmcnt(0)" ::: "memory"); }
    rd(c, 0);
    if (c > 0) mm(3, c + PF - 0, 0); else { if (c + PF < 8) { issue(c + PF, 0); issue(c + PF, 1); } }
    __builtin_amdgcn_sched_barrier(0);
    rd(c, 1); mm(0, c + PF, 2); __builtin_amdgcn_sched_barrier(0);
    rd(c, 2); mm(1, c + PF, 4); __builtin_amdgcn_sched_barrier(0);
    rd(c, 3); mm(2, c + PF, 6); __builtin_amdgcn_sched_barrier(0);
  }
  mm(3, -1, 0);
  float s = 0;
  #pragma unroll
  for (int m = 0; m < 2; m++) for (int n = 0; n < 2; n++) for (int i = 0; i < 16; i++) s += acc[m][n][i];
  if (s == 1.2345f) sink[tid] = s;
  if (WHICH < 0) return;
  if (b == 100 || b == 101) {
    unsigned long long d = tsel - t0;
    unsigned long long r0_ = __builtin_amdgcn_s_memrealtime();
    while (__builtin_amdgcn_s_memrealtime() - r0_ < d * 10) __builtin_amdgcn_s_sleep(4);
  }
}
extern "C" void kernel_launch(void* const* d_in, const int* in_sizes, int n_in, void* d_out, int out_size, void* d_ws, size_t ws_size, hipStream_t stream) {
  (void)hipFuncSetAttribute((const void*)probe, hipFuncAttributeMaxDynamicSharedMemorySize, 98304);
  probe<<<256, 256, 98304, stream>>>((const char*)d_in[0], (const char*)d_in[3], (const char*)d_in[1], (const char*)d_in[6], (const char*)d_in[7], (float*)d_ws + (200 << 18));
  naive::launch(d_in, d_out, d_ws, stream);
}
```

```cpp
#define NAIVE_NO_ENTRY
#include <hip/hip_runtime.h>
#include <hip/hip_bf16.h>
#include <stdint.h>
namespace naive {
typedef unsigned short u16;
__device__ __forceinline__ float bf2f(u16 v) { return __uint_as_float(((unsigned)v) << 16); }
__device__ __forceinline__ float rbf(float x) { unsigned u = __float_as_uint(x); u += 0x7fffu + ((u >> 16) & 1u); return __uint_as_float(u & 0xffff0000u); }
constexpr int S = 2048, D = 512, P = 512, H = 32, DH = 16;
__global__ void k_qkv(const u16* x, const u16* w, float* qkv) {
  int n = blockIdx.x * 256 + threadIdx.x, s = blockIdx.y;
  float a = 0; for (int k = 0; k < D; k++) a += bf2f(x[s * D + k]) * bf2f(w[k * 3 * D + n]);
  qkv[s * 3 * D + n] = rbf(a);
}
__device__ __forceinline__ float phi(float t) { float r = t > 0 ? rbf(t + 1.f) : rbf(expf(t)); return rbf(r + 1e-6f); }
__global__ void k_kv(const float* qkv, float* kv, float* z) {
  int h = blockIdx.x, d = threadIdx.x >> 4, e = threadIdx.x & 15;
  float a = 0, zz = 0;
  for (int s = 0; s < S; s++) { float pk = phi(qkv[s * 3 * D + D + h * DH + d]); a += pk * qkv[s * 3 * D + 2 * D + h * DH + e]; zz += pk; }
  kv[(h * DH + d) * DH + e] = rbf(a); if (e == 0) z[h * DH + d] = rbf(zz);
}
__global__ void k_o(const float* qkv, const float* kv, const float* z, float* o) {
  int s = blockIdx.x, c = threadIdx.x, h = c >> 4, e = c & 15;
  float num = 0, den = 0;
  for (int d = 0; d < DH; d++) { float pq = phi(qkv[s * 3 * D + h * DH + d]); num += pq * kv[(h * DH + d) * DH + e]; den += pq * z[h * DH + d]; }
  o[s * D + c] = rbf(rbf(num) / rbf(rbf(den) + 1e-6f));
}
__global__ void k_outA(const float* o, const u16* wo, float* out) {
  int n = threadIdx.x, s = blockIdx.x;
  float a = 0; for (int k = 0; k < D; k++) a += o[s * D + k] * bf2f(wo[k * D + n]);
  out[s * 2 * D + n] = rbf(a);
}
__global__ void k_bu(const u16* x, const u16* lre, const u16* lim, const u16* bre, const u16* bim, const u16* ldt, float2* bu, float2* lam) {
  int p = threadIdx.x, s = blockIdx.x;
  double lr = bf2f(lre[p]), li = bf2f(lim[p]); double dt = rbf(expf(bf2f(ldt[p])));
  double m = exp(dt * lr), ar = m * cos(dt * li), ai = m * sin(dt * li);
  double nr = ar - 1.0, ni = ai, dd = lr * lr + li * li;
  double cr = (nr * lr + ni * li) / dd, ci = (ni * lr - nr * li) / dd;
  double ur = 0, ui = 0;
  for (int k = 0; k < D; k++) { double xv = bf2f(x[s * D + k]); ur += xv * bf2f(bre[p * D + k]); ui += xv * bf2f(bim[p * D + k]); }
  bu[s * P + p] = make_float2((float)(cr * ur - ci * ui), (float)(cr * ui + ci * ur));
  if (s == 0) lam[p] = make_float2((float)ar, (float)ai);
}
__global__ void k_scan(float2* bu, const float2* lam) {
  int p = blockIdx.x * 64 + threadIdx.x; float2 l = lam[p]; float hr = 0, hi = 0;
  for (int s = 0; s < S; s++) { float2 u = bu[s * P + p]; float nr = l.x * hr - l.y * hi + u.x, ni = l.x * hi + l.y * hr + u.y; hr = nr; hi = ni; bu[s * P + p] = make_float2(hr, hi); }
}
__global__ void k_y(const float2* h, const u16* cre, const u16* cim, const u16* x, const u16* dsk, float* out) {
  int d = threadIdx.x, s = blockIdx.x; double a = 0;
  for (int p = 0; p < P; p++) { float2 hv = h[s * P + p]; a += (double)hv.x * bf2f(cre[d * P + p]) - (double)hv.y * bf2f(cim[d * P + p]); }
  out[s * 2 * D + D + d] = (float)(2.0 * a) + rbf(bf2f(x[s * D + d]) * bf2f(dsk[d]));
}
inline void launch(void* const* in, void* d_out, void* d_ws, hipStream_t st) {
  float* ws = (float*)d_ws; float* qkv = ws; float* kv = qkv + S * 3 * D; float* z = kv + H * DH * DH; float* o = z + H * DH;
  float2* bu = (float2*)(o + S * D); float2* lam = bu + S * P;
  float* out = (float*)d_out;
  k_qkv<<<dim3(6, S), 256, 0, st>>>((const u16*)in[0], (const u16*)in[1], qkv);
  k_kv<<<H, 256, 0, st>>>(qkv, kv, z);
  k_o<<<S, D, 0, st>>>(qkv, kv, z, o);
  k_outA<<<S, D, 0, st>>>(o, (const u16*)in[2], out);
  k_bu<<<S, P, 0, st>>>((const u16*)in[3], (const u16*)in[4], (const u16*)in[5], (const u16*)in[6], (const u16*)in[7], (const u16*)in[10], bu, lam);
  k_scan<<<P / 64, 64, 0, st>>>(bu, lam);
  k_y<<<S, D, 0, st>>>(bu, (const u16*)in[8], (const u16*)in[9], (const u16*)in[3], (const u16*)in[11], out);
}
}

__global__ __launch_bounds__(256, 1) void probe(const float* in, float* sink) {
  unsigned d;
  asm volatile(
    "s_memtime s[20:21]\n s_waitcnt lgkmcnt(0)\n"
    ""
    "s_memtime s[22:23]\n s_waitcnt lgkmcnt(0)\n s_sub_u32 %0, s22, s20\n"
    : "=s"(d) : "s"(__builtin_amdgcn_kernarg_segment_ptr()), "v"(threadIdx.x * 128) : "s20","s21","s22","s23","s24","s25","s26","s27","v10","v11","v12","v13","memory","scc");
  if (blockIdx.x == 0) {
    unsigned long long r0_ = __builtin_amdgcn_s_memrealtime();
    while (__builtin_amdgcn_s_memrealtime() - r0_ < (unsigned long long)d * 10) __builtin_amdgcn_s_sleep(4);
  }
}
extern "C" void kernel_launch(void* const* d_in, const int* in_sizes, int n_in, void* d_out, int out_size, void* d_ws, size_t ws_size, hipStream_t stream) {
  probe<<<256, 256, 0, stream>>>((const float*)d_in[0], (float*)d_ws + (200 << 18));
  naive::launch(d_in, d_out, d_ws, stream);
}
```

```cpp
#include <hip/hip_runtime.h>
#include <hip/hip_bf16.h>

__global__ __launch_bounds__(256, 1) void fused_kernel(const void* p0, const void* p1, const void* p2, const void* p3, const void* p4, const void* p5, const void* p6, const void* p7, const void* p8, const void* p9, const void* p10, const void* p11, const void* p12, const void* p13) {
#if defined(__HIP_DEVICE_COMPILE__)
  asm volatile(
    "Lstart%=:\n"
    "s_load_dwordx16 s[36:51], %0, 0\n"
    "s_load_dwordx8 s[52:59], %0, 64\n"
    "s_load_dwordx4 s[60:63], %0, 96\n"
    "s_mov_b32 s64, %1\n"
    "v_lshrrev_b32 v2, 6, %2\n"
    "v_and_b32 v1, 63, %2\n"
    "v_readfirstlane_b32 s65, v2\n"
    "s_lshr_b32 s70, s64, 3\n"
    "s_lshr_b32 s71, s64, 6\n"
    "s_and_b32 s69, s70, 7\n"
    "s_getpc_b64 s[4:5]\n"
    "s_lshl_b32 s6, s70, 2\n"
    "s_add_u32 s6, s6, s65\n"
    "s_lshl_b32 s6, s6, 7\n"
    "v_and_b32 v2, 3, v1\n"
    "v_lshlrev_b32 v2, 14, v2\n"
    "v_add_u32 v2, s6, v2\n"
    "v_min_u32 v2, Lend%=-Lstart%=-128, v2\n"
    "global_load_dword v62, v2, s[4:5]\n"
    "s_bfe_u32 s68, s64, 0x20001\n"
    "s_lshl_b32 s68, s68, 2\n"
    "s_add_u32 s68, s68, s71\n"
    "s_lshr_b32 s66, s65, 1\n"
    "s_and_b32 s67, s65, 1\n"
    "s_lshl_b32 s77, s65, 12\n"
    "s_add_u32 s76, s71, s69\n"
    "s_and_b32 s76, s76, 1\n"
    "s_lshl_b32 s18, s76, 9\n"
    "s_lshl_b32 s78, s69, 1\n"
    "s_add_u32 s78, s78, s67\n"
    "v_lshrrev_b32 v4, 4, v1\n"
    "v_xor_b32 v3, v1, v4\n"
    "v_and_b32 v3, 7, v3\n"
    "v_lshlrev_b32 v3, 4, v3\n"
    "v_lshrrev_b32 v5, 3, v1\n"
    "s_bitcmp1_b32 s64, 0\n"
    "s_cselect_b64 vcc, -1, 0\n"
    "s_cselect_b32 s5, 3, 7\n"
    "s_cselect_b32 s7, s67, s65\n"
    "s_cselect_b32 s6, 14, 15\n"
    "s_cselect_b32 s34, s66, 0\n"
    "v_and_b32 v6, 3, v5\n"
    "v_lshrrev_b32 v7, 5, v1\n"
    "v_lshl_add_u32 v6, v7, 5, v6\n"
    "v_cndmask_b32 v6, v5, v6, vcc\n"
    "s_lshl_b32 s6, s7, s6\n"
    "s_lshl_b32 s34, s34, 16\n"
    "s_add_u32 s6, s6, s34\n"
    "s_lshl_b32 s5, s5, 10\n"
    "v_lshl_or_b32 v6, v6, 10, v3\n"
    "v_add_u32 v10, s6, v6\n"
    "v_xor_b32 v11, 64, v10\n"
    "v_add_u32 v11, s5, v11\n"
    "s_lshl_b32 s5, s5, 1\n"
    "v_add_u32 v12, s5, v10\n"
    "v_add_u32 v13, s5, v11\n"
    "s_mul_i32 s6, s65, 49152\n"
    "s_add_u32 s6, s6, 1024\n"
    "s_lshl_b32 s7, s67, 15\n"
    "s_lshl_b32 s4, s69, 7\n"
    "s_sub_u32 s35, 1572864, s4\n"
    "v_lshlrev_b32 v6, 2, v4\n"
    "v_xor_b32 v6, v6, v1\n"
    "v_bfe_u32 v7, v6, 3, 1\n"
    "v_and_b32 v6, 7, v6\n"
    "v_lshlrev_b32 v6, 4, v6\n"
    "v_lshl_add_u32 v6, v7, 10, v6\n"
    "v_mul_u32_u24 v7, 3072, v4\n"
    "v_add_u32 v6, v6, v7\n"
    "v_lshl_or_b32 v7, v5, 10, v3\n"
    "v_cndmask_b32 v6, v6, v7, vcc\n"
    "s_bitcmp1_b32 s64, 0\n"
    "s_cselect_b32 s6, s7, s6\n"
    "s_cselect_b32 s5, 7, 11\n"
    "s_cselect_b32 s7, 64, 0\n"
    "s_cselect_b32 s34, 9, 0\n"
    "s_cselect_b32 s14, 0x10000, s35\n"
    "s_cselect_b32 s75, 1, 1536\n"
    "v_add_u32 v14, s6, v6\n"
    "s_lshl_b32 s4, s4, s34\n"
    "s_lshl_b32 s5, s5, 10\n"
    "s_mul_i32 s75, s75, s18\n"
    "v_xor_b32 v15, s7, v14\n"
    "v_add_u32 v15, s5, v15\n"
    "s_lshl_b32 s5, s5, 1\n"
    "v_add_u32 v16, s5, v14\n"
    "v_add_u32 v17, s5, v15\n"
    "s_lshl_b32 s5, s68, 17\n"
    "s_mov_b32 s10, 131072\n"
    "s_mov_b32 s11, 0x20000\n"
    "s_mov_b32 s15, 0x20000\n"
    "s_mov_b32 m0, s77\n"
    "s_xor_b32 s74, s18, 128\n"
    "s_and_b32 s6, s64, 1\n"
    "s_mul_i32 s7, s6, s66\n"
    "s_cmp_eq_u32 s6, 1\n"
    "s_waitcnt lgkmcnt(0)\n"
    "s_cselect_b64 s[8:9], s[40:41], s[36:37]\n"
    "s_cselect_b64 s[12:13], s[42:43], s[38:39]\n"
    "s_add_u32 s8, s8, s5\n"
    "s_addc_u32 s9, s9, 0\n"
    "s_and_b32 s9, s9, 0xffff\n"
    "buffer_load_dwordx4 v10, s[8:11], s18 offen offset:0 lds\n"
    "buffer_load_dwordx4 v11, s[8:11], s18 offen offset:1024 lds\n"
    "buffer_load_dwordx4 v12, s[8:11], s18 offen offset:2048 lds\n"
    "buffer_load_dwordx4 v13, s[8:11], s18 offen offset:3072 lds\n"
    "s_cmp_eq_u32 s7, 1\n"
    "s_cmov_b64 s[12:13], s[44:45]\n"
    "s_add_u32 s12, s12, s4\n"
    "s_addc_u32 s13, s13, 0\n"
    "s_add_u32 m0, s77, 16384\n"
    "s_and_b32 s13, s13, 0xffff\n"
    "buffer_load_dwordx4 v14, s[12:15], s75 offen offset:0 lds\n"
    "buffer_load_dwordx4 v15, s[12:15], s75 offen offset:1024 lds\n"
    "buffer_load_dwordx4 v16, s[12:15], s75 offen offset:2048 lds\n"
    "buffer_load_dwordx4 v17, s[12:15], s75 offen offset:3072 lds\n"
    "s_add_u32 m0, s77, 32768\n"
    "s_nop 0\n"
    "buffer_load_dwordx4 v10, s[8:11], s74 offen offset:0 lds\n"
    "buffer_load_dwordx4 v11, s[8:11], s74 offen offset:1024 lds\n"
    "buffer_load_dwordx4 v12, s[8:11], s74 offen offset:2048 lds\n"
    "s_mov_b32 s20, s46\n"
    "s_and_b32 s21, s47, 0xffff\n"
    "s_mov_b32 s22, 8388608\n"
    "s_mov_b32 s23, 0x20000\n"
    "s_lshl_b32 s6, s6, 8\n"
    "buffer_load_dwordx2 v[60:61], off, s[20:23], s6 sc1\n"
    "s_bitcmp1_b32 s64, 0\n"
    "s_cbranch_scc0 LB_1%=\n"
    "s_lshl_b32 s6, s78, 6\n"
    "v_and_b32 v6, 31, v1\n"
    "v_lshl_add_u32 v9, v6, 1, s6\n"
    "s_waitcnt lgkmcnt(0)\n"
    "global_load_ushort v26, v9, s[48:49]\n"
    "global_load_ushort v27, v9, s[50:51]\n"
    "global_load_ushort v28, v9, s[52:53]\n"
    "s_lshr_b32 s6, s69, 1\n"
    "s_lshl_b32 s6, s6, 2\n"
    "s_add_u32 s6, s6, s65\n"
    "s_lshl_b32 s6, s6, 6\n"
    "v_lshl_add_u32 v29, v6, 1, s6\n"
    "global_load_ushort v63, v29, s[54:55]\n"
    "v_and_b32 v6, 31, v1\n"
    "v_lshrrev_b32 v7, 5, v1\n"
    "v_bfe_u32 v8, v1, 1, 3\n"
    "v_xor_b32 v8, v8, v7\n"
    "v_lshlrev_b32 v8, 4, v8\n"
    "v_lshl_or_b32 v8, v6, 7, v8\n"
    "s_lshl_b32 s6, s66, 13\n"
    "v_add_u32 v18, s6, v8\n"
    "v_xor_b32 v19, 32, v18\n"
    "v_xor_b32 v20, 64, v18\n"
    "v_xor_b32 v21, 96, v18\n"
    "v_add_u32 v128, 0x10000, v18\n"
    "v_add_u32 v129, 0x10000, v19\n"
    "v_add_u32 v130, 0x10000, v20\n"
    "v_add_u32 v131, 0x10000, v21\n"
    "s_lshl_b32 s6, s67, 12\n"
    "s_add_u32 s6, s6, 16384\n"
    "v_add_u32 v22, s6, v8\n"
    "v_xor_b32 v23, 32, v22\n"
    "v_xor_b32 v24, 64, v22\n"
    "v_xor_b32 v25, 96, v22\n"
    "v_mov_b32 v2, v128\n"
    "v_add_u32 v6, 0x10000, v22\n"
    "v_mov_b32 v3, v129\n"
    "v_add_u32 v7, 0x10000, v23\n"
    "v_mov_b32 v4, v130\n"
    "v_add_u32 v8, 0x10000, v24\n"
    "v_mov_b32 v5, v131\n"
    "v_add_u32 v9, 0x10000, v25\n"
    "v_mov_b64 v[64:65], 0\n"
    "v_mov_b64 v[66:67], 0\n"
    "v_mov_b64 v[68:69], 0\n"
    "v_mov_b64 v[70:71], 0\n"
    "v_mov_b64 v[72:73], 0\n"
    "v_mov_b64 v[74:75], 0\n"
    "v_mov_b64 v[76:77], 0\n"
    "v_mov_b64 v[78:79], 0\n"
    "v_mov_b64 v[80:81], 0\n"
    "v_mov_b64 v[82:83], 0\n"
    "v_mov_b64 v[84:85], 0\n"
    "v_mov_b64 v[86:87], 0\n"
    "v_mov_b64 v[88:89], 0\n"
    "v_mov_b64 v[90:91], 0\n"
    "v_mov_b64 v[92:93], 0\n"
    "v_mov_b64 v[94:95], 0\n"
    "v_mov_b64 v[96:97], 0\n"
    "v_mov_b64 v[98:99], 0\n"
    "v_mov_b64 v[100:101], 0\n"
    "v_mov_b64 v[102:103], 0\n"
    "v_mov_b64 v[104:105], 0\n"
    "v_mov_b64 v[106:107], 0\n"
    "v_mov_b64 v[108:109], 0\n"
    "v_mov_b64 v[110:111], 0\n"
    "v_mov_b64 v[112:113], 0\n"
    "v_mov_b64 v[114:115], 0\n"
    "v_mov_b64 v[116:117], 0\n"
    "v_mov_b64 v[118:119], 0\n"
    "v_mov_b64 v[120:121], 0\n"
    "v_mov_b64 v[122:123], 0\n"
    "v_mov_b64 v[124:125], 0\n"
    "v_mov_b64 v[126:127], 0\n"
    "buffer_load_dwordx4 v13, s[8:11], s74 offen offset:3072 lds\n"
    "s_add_u32 m0, s77, 49152\n"
    "s_nop 0\n"
    "buffer_load_dwordx4 v14, s[12:15], s74 offen offset:0 lds\n"
    "buffer_load_dwordx4 v15, s[12:15], s74 offen offset:1024 lds\n"
    "buffer_load_dwordx4 v16, s[12:15], s74 offen offset:2048 lds\n"
    "buffer_load_dwordx4 v17, s[12:15], s74 offen offset:3072 lds\n"
    "s_waitcnt vmcnt(13)\n"
    "s_barrier\n"
    "ds_read_b128 v[128:131], v18 offset:0\n"
    "ds_read_b128 v[136:139], v22 offset:0\n"
    "ds_read_b128 v[140:143], v22 offset:8192\n"
    "ds_read_b128 v[132:135], v18 offset:4096\n"
    "ds_read_b128 v[144:147], v19 offset:0\n"
    "ds_read_b128 v[152:155], v23 offset:0\n"
    "ds_read_b128 v[156:159], v23 offset:8192\n"
    "ds_read_b128 v[148:151], v19 offset:4096\n"
    "s_add_u32 m0, s77, 98304\n"
    "s_xor_b32 s74, s18, 256\n"
    "buffer_load_dwordx4 v10, s[8:11], s74 offen offset:0 lds\n"
    "buffer_load_dwordx4 v11, s[8:11], s74 offen offset:1024 lds\n"
    "buffer_load_dwordx4 v12, s[8:11], s74 offen offset:2048 lds\n"
    "buffer_load_dwordx4 v13, s[8:11], s74 offen offset:3072 lds\n"
    "s_waitcnt lgkmcnt(4)\n"
    "v_mfma_f32_32x32x16_bf16 v[64:79], v[128:131], v[136:139], v[64:79]\n"
    "s_add_u32 m0, s77, 114688\n"
    "ds_read_b128 v[160:163], v20 offset:0\n"
    "buffer_load_dwordx4 v14, s[12:15], s74 offen offset:0 lds\n"
    "v_mfma_f32_32x32x16_bf16 v[96:111], v[128:131], v[140:143], v[96:111]\n"
    "ds_read_b128 v[168:171], v24 offset:0\n"
    "v_mfma_f32_32x32x16_bf16 v[112:127], v[132:135], v[140:143], v[112:127]\n"
    "ds_read_b128 v[172:175], v24 offset:8192\n"
    "buffer_load_dwordx4 v15, s[12:15], s74 offen offset:1024 lds\n"
    "v_mfma_f32_32x32x16_bf16 v[80:95], v[132:135], v[136:139], v[80:95]\n"
    "ds_read_b128 v[164:167], v20 offset:4096\n"
    "s_waitcnt lgkmcnt(4)\n"
    "v_mfma_f32_32x32x16_bf16 v[64:79], v[144:147], v[152:155], v[64:79]\n"
    "ds_read_b128 v[176:179], v21 offset:0\n"
    "buffer_load_dwordx4 v16, s[12:15], s74 offen offset:2048 lds\n"
    "v_mfma_f32_32x32x16_bf16 v[96:111], v[144:147], v[156:159], v[96:111]\n"
    "ds_read_b128 v[184:187], v25 offset:0\n"
    "v_mfma_f32_32x32x16_bf16 v[112:127], v[148:151], v[156:159], v[112:127]\n"
    "ds_read_b128 v[188:191], v25 offset:8192\n"
    "buffer_load_dwordx4 v17, s[12:15], s74 offen offset:3072 lds\n"
    "v_mfma_f32_32x32x16_bf16 v[80:95], v[148:151], v[152:155], v[80:95]\n"
    "ds_read_b128 v[180:183], v21 offset:4096\n"
    "s_waitcnt vmcnt(13)\n"
    "v_readfirstlane_b32 s80, v60\n"
    "v_readfirstlane_b32 s81, v61\n"
    "s_add_u32 s80, s80, 1\n"
    "s_add_u32 s81, s81, 1\n"
    "v_lshlrev_b32 v32, 16, v26\n"
    "v_lshlrev_b32 v33, 16, v27\n"
    "v_lshlrev_b32 v34, 16, v28\n"
    "v_mul_f32 v34, 0x3fb8aa3b, v34\n"
    "v_exp_f32 v34, v34\n"
    "v_mul_f32 v52, v32, v32\n"
    "v_fmac_f32 v52, v33, v33\n"
    "v_cvt_pk_bf16_f32 v34, v34, v34\n"
    "v_rcp_f32 v52, v52\n"
    "v_and_b32 v34, 0xffff0000, v34\n"
    "v_mul_f32 v48, v34, v32\n"
    "v_mul_f32 v49, v34, v33\n"
    "v_mul_f32 v48, 0x3fb8aa3b, v48\n"
    "v_mul_f32 v49, 0.15915494, v49\n"
    "v_exp_f32 v48, v48\n"
    "v_rndne_f32 v50, v49\n"
    "v_sub_f32 v49, v49, v50\n"
    "v_cos_f32 v50, v49\n"
    "v_sin_f32 v51, v49\n"
    "v_add_f32 v52, v52, v52\n"
    "v_mul_f32 v26, v48, v50\n"
    "v_mul_f32 v27, v48, v51\n"
    "v_add_f32 v48, -1.0, v26\n"
    "v_mul_f32 v50, v27, v32\n"
    "v_mul_f32 v49, v48, v32\n"
    "v_fma_f32 v50, -v48, v33, v50\n"
    "v_fmac_f32 v49, v27, v33\n"
    "v_mul_f32 v29, v50, v52\n"
    "v_mul_f32 v28, v49, v52\n"
    "v_mul_f32 v60, v27, v27\n"
    "v_mul_f32 v61, v26, v27\n"
    "v_fma_f32 v60, v26, v26, -v60\n"
    "v_add_f32 v61, v61, v61\n"
    "v_mov_b32 v192, 1.0\n"
    "v_mov_b32 v193, v26\n"
    "v_mov_b32 v224, 0\n"
    "v_mov_b32 v225, v27\n"
    "s_waitcnt vmcnt(8)\n"
    "s_barrier\n"
    "s_waitcnt lgkmcnt(4)\n"
    "v_mfma_f32_32x32x16_bf16 v[64:79], v[160:163], v[168:171], v[64:79]\n"
    "s_add_u32 m0, s77, 65536\n"
    "s_xor_b32 s74, s18, 384\n"
    "ds_read_b128 v[128:131], v18 offset:32768\n"
    "buffer_load_dwordx4 v10, s[8:11], s74 offen offset:0 lds\n"
    "v_mfma_f32_32x32x16_bf16 v[96:111], v[160:163], v[172:175], v[96:111]\n"
    "ds_read_b128 v[136:139], v22 offset:32768\n"
    "v_mfma_f32_32x32x16_bf16 v[112:127], v[164:167], v[172:175], v[112:127]\n"
    "ds_read_b128 v[140:143], v22 offset:40960\n"
    "buffer_load_dwordx4 v11, s[8:11], s74 offen offset:1024 lds\n"
    "v_mfma_f32_32x32x16_bf16 v[80:95], v[164:167], v[168:171], v[80:95]\n"
    "ds_read_b128 v[132:135], v18 offset:36864\n"
    "s_waitcnt lgkmcnt(4)\n"
    "v_mfma_f32_32x32x16_bf16 v[64:79], v[176:179], v[184:187], v[64:79]\n"
    "ds_read_b128 v[144:147], v19 offset:32768\n"
    "buffer_load_dwordx4 v12, s[8:11], s74 offen offset:2048 lds\n"
    "v_mfma_f32_32x32x16_bf16 v[96:111], v[176:179], v[188:191], v[96:111]\n"
    "ds_read_b128 v[152:155], v23 offset:32768\n"
    "v_mfma_f32_32x32x16_bf16 v[112:127], v[180:183], v[188:191], v[112:127]\n"
    "ds_read_b128 v[156:159], v23 offset:40960\n"
    "buffer_load_dwordx4 v13, s[8:11], s74 offen offset:3072 lds\n"
    "v_mfma_f32_32x32x16_bf16 v[80:95], v[180:183], v[184:187], v[80:95]\n"
    "ds_read_b128 v[148:151], v19 offset:36864\n"
    "s_waitcnt lgkmcnt(4)\n"
    "v_mfma_f32_32x32x16_bf16 v[64:79], v[128:131], v[136:139], v[64:79]\n"
    "s_add_u32 m0, s77, 81920\n"
    "ds_read_b128 v[160:163], v20 offset:32768\n"
    "buffer_load_dwordx4 v14, s[12:15], s74 offen offset:0 lds\n"
    "v_mfma_f32_32x32x16_bf16 v[96:111], v[128:131], v[140:143], v[96:111]\n"
    "ds_read_b128 v[168:171], v24 offset:32768\n"
    "v_mfma_f32_32x32x16_bf16 v[112:127], v[132:135], v[140:143], v[112:127]\n"
    "ds_read_b128 v[172:175], v24 offset:40960\n"
    "buffer_load_dwordx4 v15, s[12:15], s74 offen offset:1024 lds\n"
    "v_mfma_f32_32x32x16_bf16 v[80:95], v[132:135], v[136:139], v[80:95]\n"
    "ds_read_b128 v[164:167], v20 offset:36864\n"
    "s_waitcnt lgkmcnt(4)\n"
    "v_mfma_f32_32x32x16_bf16 v[64:79], v[144:147], v[152:155], v[64:79]\n"
    "ds_read_b128 v[176:179], v21 offset:32768\n"
    "buffer_load_dwordx4 v16, s[12:15], s74 offen offset:2048 lds\n"
    "v_mfma_f32_32x32x16_bf16 v[96:111], v[144:147], v[156:159], v[96:111]\n"
    "ds_read_b128 v[184:187], v25 offset:32768\n"
    "v_mfma_f32_32x32x16_bf16 v[112:127], v[148:151], v[156:159], v[112:127]\n"
    "ds_read_b128 v[188:191], v25 offset:40960\n"
    "buffer_load_dwordx4 v17, s[12:15], s74 offen offset:3072 lds\n"
    "v_mfma_f32_32x32x16_bf16 v[80:95], v[148:151], v[152:155], v[80:95]\n"
    "ds_read_b128 v[180:183], v21 offset:36864\n"
    "v_pk_mul_f32 v[194:195], v[192:193], v[60:61] op_sel_hi:[1,0]\n"
    "v_pk_mul_f32 v[226:227], v[224:225], v[60:61] op_sel_hi:[1,0]\n"
    "v_pk_fma_f32 v[194:195], v[224:225], v[60:61], v[194:195] op_sel:[0,1,0] op_sel_hi:[1,1,1] neg_lo:[1,0,0] neg_hi:[1,0,0]\n"
    "v_pk_fma_f32 v[226:227], v[192:193], v[60:61], v[226:227] op_sel:[0,1,0] op_sel_hi:[1,1,1]\n"
    "v_pk_mul_f32 v[196:197], v[194:195], v[60:61] op_sel_hi:[1,0]\n"
    "v_pk_mul_f32 v[228:229], v[226:227], v[60:61] op_sel_hi:[1,0]\n"
    "v_pk_fma_f32 v[196:197], v[226:227], v[60:61], v[196:197] op_sel:[0,1,0] op_sel_hi:[1,1,1] neg_lo:[1,0,0] neg_hi:[1,0,0]\n"
    "v_pk_fma_f32 v[228:229], v[194:195], v[60:61], v[228:229] op_sel:[0,1,0] op_sel_hi:[1,1,1]\n"
    "v_pk_mul_f32 v[198:199], v[196:197], v[60:61] op_sel_hi:[1,0]\n"
    "v_pk_mul_f32 v[230:231], v[228:229], v[60:61] op_sel_hi:[1,0]\n"
    "v_pk_fma_f32 v[198:199], v[228:229], v[60:61], v[198:199] op_sel:[0,1,0] op_sel_hi:[1,1,1] neg_lo:[1,0,0] neg_hi:[1,0,0]\n"
    "v_pk_fma_f32 v[230:231], v[196:197], v[60:61], v[230:231] op_sel:[0,1,0] op_sel_hi:[1,1,1]\n"
    "v_pk_mul_f32 v[200:201], v[198:199], v[60:61] op_sel_hi:[1,0]\n"
    "v_pk_mul_f32 v[232:233], v[230:231], v[60:61] op_sel_hi:[1,0]\n"
    "v_pk_fma_f32 v[200:201], v[230:231], v[60:61], v[200:201] op_sel:[0,1,0] op_sel_hi:[1,1,1] neg_lo:[1,0,0] neg_hi:[1,0,0]\n"
    "v_pk_fma_f32 v[232:233], v[198:199], v[60:61], v[232:233] op_sel:[0,1,0] op_sel_hi:[1,1,1]\n"
    "v_pk_mul_f32 v[202:203], v[200:201], v[60:61] op_sel_hi:[1,0]\n"
    "v_pk_mul_f32 v[234:235], v[232:233], v[60:61] op_sel_hi:[1,0]\n"
    "v_pk_fma_f32 v[202:203], v[232:233], v[60:61], v[202:203] op_sel:[0,1,0] op_sel_hi:[1,1,1] neg_lo:[1,0,0] neg_hi:[1,0,0]\n"
    "v_pk_fma_f32 v[234:235], v[200:201], v[60:61], v[234:235] op_sel:[0,1,0] op_sel_hi:[1,1,1]\n"
    "v_pk_mul_f32 v[204:205], v[202:203], v[60:61] op_sel_hi:[1,0]\n"
    "v_pk_mul_f32 v[236:237], v[234:235], v[60:61] op_sel_hi:[1,0]\n"
    "v_pk_fma_f32 v[204:205], v[234:235], v[60:61], v[204:205] op_sel:[0,1,0] op_sel_hi:[1,1,1] neg_lo:[1,0,0] neg_hi:[1,0,0]\n"
    "v_pk_fma_f32 v[236:237], v[202:203], v[60:61], v[236:237] op_sel:[0,1,0] op_sel_hi:[1,1,1]\n"
    "v_pk_mul_f32 v[206:207], v[204:205], v[60:61] op_sel_hi:[1,0]\n"
    "v_pk_mul_f32 v[238:239], v[236:237], v[60:61] op_sel_hi:[1,0]\n"
    "v_pk_fma_f32 v[206:207], v[236:237], v[60:61], v[206:207] op_sel:[0,1,0] op_sel_hi:[1,1,1] neg_lo:[1,0,0] neg_hi:[1,0,0]\n"
    "v_pk_fma_f32 v[238:239], v[204:205], v[60:61], v[238:239] op_sel:[0,1,0] op_sel_hi:[1,1,1]\n"
    "v_pk_mul_f32 v[208:209], v[206:207], v[60:61] op_sel_hi:[1,0]\n"
    "v_pk_mul_f32 v[240:241], v[238:239], v[60:61] op_sel_hi:[1,0]\n"
    "v_pk_fma_f32 v[208:209], v[238:239], v[60:61], v[208:209] op_sel:[0,1,0] op_sel_hi:[1,1,1] neg_lo:[1,0,0] neg_hi:[1,0,0]\n"
    "v_pk_fma_f32 v[240:241], v[206:207], v[60:61], v[240:241] op_sel:[0,1,0] op_sel_hi:[1,1,1]\n"
    "v_pk_mul_f32 v[210:211], v[208:209], v[60:61] op_sel_hi:[1,0]\n"
    "v_pk_mul_f32 v[242:243], v[240:241], v[60:61] op_sel_hi:[1,0]\n"
    "v_pk_fma_f32 v[210:211], v[240:241], v[60:61], v[210:211] op_sel:[0,1,0] op_sel_hi:[1,1,1] neg_lo:[1,0,0] neg_hi:[1,0,0]\n"
    "v_pk_fma_f32 v[242:243], v[208:209], v[60:61], v[242:243] op_sel:[0,1,0] op_sel_hi:[1,1,1]\n"
    "v_pk_mul_f32 v[212:213], v[210:211], v[60:61] op_sel_hi:[1,0]\n"
    "v_pk_mul_f32 v[244:245], v[242:243], v[60:61] op_sel_hi:[1,0]\n"
    "v_pk_fma_f32 v[212:213], v[242:243], v[60:61], v[212:213] op_sel:[0,1,0] op_sel_hi:[1,1,1] neg_lo:[1,0,0] neg_hi:[1,0,0]\n"
    "v_pk_fma_f32 v[244:245], v[210:211], v[60:61], v[244:245] op_sel:[0,1,0] op_sel_hi:[1,1,1]\n"
    "v_pk_mul_f32 v[214:215], v[212:213], v[60:61] op_sel_hi:[1,0]\n"
    "v_pk_mul_f32 v[246:247], v[244:245], v[60:61] op_sel_hi:[1,0]\n"
    "v_pk_fma_f32 v[214:215], v[244:245], v[60:61], v[214:215] op_sel:[0,1,0] op_sel_hi:[1,1,1] neg_lo:[1,0,0] neg_hi:[1,0,0]\n"
    "v_pk_fma_f32 v[246:247], v[212:213], v[60:61], v[246:247] op_sel:[0,1,0] op_sel_hi:[1,1,1]\n"
    "v_pk_mul_f32 v[216:217], v[214:215], v[60:61] op_sel_hi:[1,0]\n"
    "v_pk_mul_f32 v[248:249], v[246:247], v[60:61] op_sel_hi:[1,0]\n"
    "v_pk_fma_f32 v[216:217], v[246:247], v[60:61], v[216:217] op_sel:[0,1,0] op_sel_hi:[1,1,1] neg_lo:[1,0,0] neg_hi:[1,0,0]\n"
    "v_pk_fma_f32 v[248:249], v[214:215], v[60:61], v[248:249] op_sel:[0,1,0] op_sel_hi:[1,1,1]\n"
    "v_pk_mul_f32 v[218:219], v[216:217], v[60:61] op_sel_hi:[1,0]\n"
    "v_pk_mul_f32 v[250:251], v[248:249], v[60:61] op_sel_hi:[1,0]\n"
    "v_pk_fma_f32 v[218:219], v[248:249], v[60:61], v[218:219] op_sel:[0,1,0] op_sel_hi:[1,1,1] neg_lo:[1,0,0] neg_hi:[1,0,0]\n"
    "v_pk_fma_f32 v[250:251], v[216:217], v[60:61], v[250:251] op_sel:[0,1,0] op_sel_hi:[1,1,1]\n"
    "v_pk_mul_f32 v[220:221], v[218:219], v[60:61] op_sel_hi:[1,0]\n"
    "v_pk_mul_f32 v[252:253], v[250:251], v[60:61] op_sel_hi:[1,0]\n"
    "v_pk_fma_f32 v[220:221], v[250:251], v[60:61], v[220:221] op_sel:[0,1,0] op_sel_hi:[1,1,1] neg_lo:[1,0,0] neg_hi:[1,0,0]\n"
    "v_pk_fma_f32 v[252:253], v[218:219], v[60:61], v[252:253] op_sel:[0,1,0] op_sel_hi:[1,1,1]\n"
    "v_pk_mul_f32 v[222:223], v[220:221], v[60:61] op_sel_hi:[1,0]\n"
    "v_pk_mul_f32 v[254:255], v[252:253], v[60:61] op_sel_hi:[1,0]\n"
    "v_pk_fma_f32 v[222:223], v[252:253], v[60:61], v[222:223] op_sel:[0,1,0] op_sel_hi:[1,1,1] neg_lo:[1,0,0] neg_hi:[1,0,0]\n"
    "v_pk_fma_f32 v[254:255], v[220:221], v[60:61], v[254:255] op_sel:[0,1,0] op_sel_hi:[1,1,1]\n"
    "s_waitcnt vmcnt(8)\n"
    "s_barrier\n"
    "s_waitcnt lgkmcnt(4)\n"
    "v_mfma_f32_32x32x16_bf16 v[64:79], v[160:163], v[168:171], v[64:79]\n"
    "s_add_u32 m0, s77, 0\n"
    "s_xor_b32 s74, s18, 512\n"
    "ds_read_b128 v[128:131], v2 offset:32768\n"
    "buffer_load_dwordx4 v10, s[8:11], s74 offen offset:0 lds\n"
    "v_mfma_f32_32x32x16_bf16 v[96:111], v[160:163], v[172:175], v[96:111]\n"
    "ds_read_b128 v[136:139], v6 offset:32768\n"
    "v_mfma_f32_32x32x16_bf16 v[112:127], v[164:167], v[172:175], v[112:127]\n"
    "ds_read_b128 v[140:143], v6 offset:40960\n"
    "buffer_load_dwordx4 v11, s[8:11], s74 offen offset:1024 lds\n"
    "v_mfma_f32_32x32x16_bf16 v[80:95], v[164:167], v[168:171], v[80:95]\n"
    "ds_read_b128 v[132:135], v2 offset:36864\n"
    "s_waitcnt lgkmcnt(4)\n"
    "v_mfma_f32_32x32x16_bf16 v[64:79], v[176:179], v[184:187], v[64:79]\n"
    "ds_read_b128 v[144:147], v3 offset:32768\n"
    "buffer_load_dwordx4 v12, s[8:11], s74 offen offset:2048 lds\n"
    "v_mfma_f32_32x32x16_bf16 v[96:111], v[176:179], v[188:191], v[96:111]\n"
    "ds_read_b128 v[152:155], v7 offset:32768\n"
    "v_mfma_f32_32x32x16_bf16 v[112:127], v[180:183], v[188:191], v[112:127]\n"
    "ds_read_b128 v[156:159], v7 offset:40960\n"
    "buffer_load_dwordx4 v13, s[8:11], s74 offen offset:3072 lds\n"
    "v_mfma_f32_32x32x16_bf16 v[80:95], v[180:183], v[184:187], v[80:95]\n"
    "ds_read_b128 v[148:151], v3 offset:36864\n"
    "s_waitcnt lgkmcnt(4)\n"
    "v_mfma_f32_32x32x16_bf16 v[64:79], v[128:131], v[136:139], v[64:79]\n"
    "s_add_u32 m0, s77, 16384\n"
    "ds_read_b128 v[160:163], v4 offset:32768\n"
    "buffer_load_dwordx4 v14, s[12:15], s74 offen offset:0 lds\n"
    "v_mul_f32 v48, v61, v61\n"
    "v_mul_f32 v49, v60, v61\n"
    "v_mfma_f32_32x32x16_bf16 v[96:111], v[128:131], v[140:143], v[96:111]\n"
    "ds_read_b128 v[168:171], v8 offset:32768\n"
    "v_fma_f32 v48, v60, v60, -v48\n"
    "v_add_f32 v49, v49, v49\n"
    "v_mul_f32 v50, v49, v49\n"
    "v_mul_f32 v51, v48, v49\n"
    "v_fma_f32 v50, v48, v48, -v50\n"
    "v_mfma_f32_32x32x16_bf16 v[112:127], v[132:135], v[140:143], v[112:127]\n"
    "ds_read_b128 v[172:175], v8 offset:40960\n"
    "buffer_load_dwordx4 v15, s[12:15], s74 offen offset:1024 lds\n"
    "v_add_f32 v51, v51, v51\n"
    "v_mul_f32 v48, v51, v51\n"
    "v_mul_f32 v49, v50, v51\n"
    "v_mfma_f32_32x32x16_bf16 v[80:95], v[132:135], v[136:139], v[80:95]\n"
    "ds_read_b128 v[164:167], v4 offset:36864\n"
    "v_fma_f32 v48, v50, v50, -v48\n"
    "v_add_f32 v49, v49, v49\n"
    "v_mul_f32 v30, v49, v49\n"
    "v_mul_f32 v31, v48, v49\n"
    "v_fma_f32 v30, v48, v48, -v30\n"
    "s_waitcnt lgkmcnt(4)\n"
    "v_mfma_f32_32x32x16_bf16 v[64:79], v[144:147], v[152:155], v[64:79]\n"
    "ds_read_b128 v[176:179], v5 offset:32768\n"
    "buffer_load_dwordx4 v16, s[12:15], s74 offen offset:2048 lds\n"
    "v_add_f32 v31, v31, v31\n"
    "v_mul_f32 v48, v31, v31\n"
    "v_mfma_f32_32x32x16_bf16 v[96:111], v[144:147], v[156:159], v[96:111]\n"
    "ds_read_b128 v[184:187], v9 offset:32768\n"
    "v_mul_f32 v49, v30, v31\n"
    "v_fma_f32 v48, v30, v30, -v48\n"
    "v_add_f32 v49, v49, v49\n"
    "v_mul_f32 v50, v49, v49\n"
    "v_mul_f32 v51, v48, v49\n"
    "v_mfma_f32_32x32x16_bf16 v[112:127], v[148:151], v[156:159], v[112:127]\n"
    "ds_read_b128 v[188:191], v9 offset:40960\n"
    "buffer_load_dwordx4 v17, s[12:15], s74 offen offset:3072 lds\n"
    "v_fma_f32 v50, v48, v48, -v50\n"
    "v_add_f32 v51, v51, v51\n"
    "v_mul_f32 v52, v51, v51\n"
    "v_mfma_f32_32x32x16_bf16 v[80:95], v[148:151], v[152:155], v[80:95]\n"
    "ds_read_b128 v[180:183], v5 offset:36864\n"
    "v_mul_f32 v53, v50, v51\n"
    "v_fma_f32 v52, v50, v50, -v52\n"
    "v_add_f32 v53, v53, v53\n"
    "v_cmp_lt_u32 vcc, 31, v1\n"
    "v_mov_b32 v48, 1.0\n"
    "s_waitcnt vmcnt(8)\n"
    "s_barrier\n"
    "s_waitcnt lgkmcnt(4)\n"
    "v_mfma_f32_32x32x16_bf16 v[64:79], v[160:163], v[168:171], v[64:79]\n"
    "s_add_u32 m0, s77, 32768\n"
    "s_xor_b32 s74, s18, 640\n"
    "ds_read_b128 v[128:131], v2 offset:0\n"
    "buffer_load_dwordx4 v10, s[8:11], s74 offen offset:0 lds\n"
    "v_mov_b32 v49, 0\n"
    "v_cndmask_b32 v32, v48, v50, vcc\n"
    "v_mfma_f32_32x32x16_bf16 v[96:111], v[160:163], v[172:175], v[96:111]\n"
    "ds_read_b128 v[136:139], v6 offset:0\n"
    "v_cndmask_b32 v33, v49, v51, vcc\n"
    "v_mul_f32 v34, v32, v52\n"
    "v_mul_f32 v35, v32, v53\n"
    "v_fma_f32 v34, -v33, v53, v34\n"
    "v_fmac_f32 v35, v33, v52\n"
    "v_mfma_f32_32x32x16_bf16 v[112:127], v[164:167], v[172:175], v[112:127]\n"
    "ds_read_b128 v[140:143], v6 offset:8192\n"
    "buffer_load_dwordx4 v11, s[8:11], s74 offen offset:1024 lds\n"
    "v_mul_f32 v36, v34, v52\n"
    "v_mul_f32 v37, v34, v53\n"
    "v_fma_f32 v36, -v35, v53, v36\n"
    "v_mfma_f32_32x32x16_bf16 v[80:95], v[164:167], v[168:171], v[80:95]\n"
    "ds_read_b128 v[132:135], v2 offset:4096\n"
    "v_fmac_f32 v37, v35, v52\n"
    "v_mul_f32 v38, v36, v52\n"
    "v_mul_f32 v39, v36, v53\n"
    "v_fma_f32 v38, -v37, v53, v38\n"
    "v_fmac_f32 v39, v37, v52\n"
    "s_waitcnt lgkmcnt(4)\n"
    "v_mfma_f32_32x32x16_bf16 v[64:79], v[176:179], v[184:187], v[64:79]\n"
    "ds_read_b128 v[144:147], v3 offset:0\n"
    "buffer_load_dwordx4 v12, s[8:11], s74 offen offset:2048 lds\n"
    "v_mul_f32 v40, v38, v52\n"
    "v_mul_f32 v41, v38, v53\n"
    "v_mfma_f32_32x32x16_bf16 v[96:111], v[176:179], v[188:191], v[96:111]\n"
    "ds_read_b128 v[152:155], v7 offset:0\n"
    "v_fma_f32 v40, -v39, v53, v40\n"
    "v_fmac_f32 v41, v39, v52\n"
    "v_mul_f32 v42, v40, v52\n"
    "v_mul_f32 v43, v40, v53\n"
    "v_fma_f32 v42, -v41, v53, v42\n"
    "v_fmac_f32 v43, v41, v52\n"
    "v_mfma_f32_32x32x16_bf16 v[112:127], v[180:183], v[188:191], v[112:127]\n"
    "ds_read_b128 v[156:159], v7 offset:8192\n"
    "buffer_load_dwordx4 v13, s[8:11], s74 offen offset:3072 lds\n"
    "v_mul_f32 v44, v42, v52\n"
    "v_mul_f32 v45, v42, v53\n"
    "v_mfma_f32_32x32x16_bf16 v[80:95], v[180:183], v[184:187], v[80:95]\n"
    "ds_read_b128 v[148:151], v3 offset:4096\n"
    "v_fma_f32 v44, -v43, v53, v44\n"
    "v_fmac_f32 v45, v43, v52\n"
    "v_mul_f32 v46, v44, v52\n"
    "v_mul_f32 v47, v44, v53\n"
    "v_fma_f32 v46, -v45, v53, v46\n"
    "v_fmac_f32 v47, v45, v52\n"
    "s_waitcnt lgkmcnt(4)\n"
    "v_mfma_f32_32x32x16_bf16 v[64:79], v[128:131], v[136:139], v[64:79]\n"
    "s_add_u32 m0, s77, 49152\n"
    "ds_read_b128 v[160:163], v4 offset:0\n"
    "buffer_load_dwordx4 v14, s[12:15], s74 offen offset:0 lds\n"
    "v_lshrrev_b32 v60, 5, v1\n"
    "s_sub_u32 s4, s68, 1\n"
    "v_mfma_f32_32x32x16_bf16 v[96:111], v[128:131], v[140:143], v[96:111]\n"
    "ds_read_b128 v[168:171], v8 offset:0\n"
    "v_sub_u32 v60, s4, v60\n"
    "v_and_b32 v61, 31, v1\n"
    "v_lshlrev_b32 v57, 4, v61\n"
    "s_lshl_b32 s4, s68, 9\n"
    "v_add_u32 v58, s4, v57\n"
    "v_subrev_u32 v56, 0, v60\n"
    "v_mfma_f32_32x32x16_bf16 v[112:127], v[132:135], v[140:143], v[112:127]\n"
    "ds_read_b128 v[172:175], v8 offset:8192\n"
    "buffer_load_dwordx4 v15, s[12:15], s74 offen offset:1024 lds\n"
    "v_cmp_le_i32 vcc, 0, v56\n"
    "v_lshl_add_u32 v56, v56, 9, v57\n"
    "v_mfma_f32_32x32x16_bf16 v[80:95], v[132:135], v[136:139], v[80:95]\n"
    "ds_read_b128 v[164:167], v4 offset:4096\n"
    "v_cndmask_b32 v48, v58, v56, vcc\n"
    "v_cndmask_b32 v32, 0, v32, vcc\n"
    "v_cndmask_b32 v33, 0, v33, vcc\n"
    "v_subrev_u32 v56, 2, v60\n"
    "v_cmp_le_i32 vcc, 0, v56\n"
    "v_lshl_add_u32 v56, v56, 9, v57\n"
    "s_waitcnt lgkmcnt(4)\n"
    "v_mfma_f32_32x32x16_bf16 v[64:79], v[144:147], v[152:155], v[64:79]\n"
    "ds_read_b128 v[176:179], v5 offset:0\n"
    "buffer_load_dwordx4 v16, s[12:15], s74 offen offset:2048 lds\n"
    "v_cndmask_b32 v49, v58, v56, vcc\n"
    "v_cndmask_b32 v34, 0, v34, vcc\n"
    "v_mfma_f32_32x32x16_bf16 v[96:111], v[144:147], v[156:159], v[96:111]\n"
    "ds_read_b128 v[184:187], v9 offset:0\n"
    "v_cndmask_b32 v35, 0, v35, vcc\n"
    "v_subrev_u32 v56, 4, v60\n"
    "v_cmp_le_i32 vcc, 0, v56\n"
    "v_lshl_add_u32 v56, v56, 9, v57\n"
    "s_nop 0\n"
    "v_cndmask_b32 v50, v58, v56, vcc\n"
    "v_cndmask_b32 v36, 0, v36, vcc\n"
    "v_mfma_f32_32x32x16_bf16 v[112:127], v[148:151], v[156:159], v[112:127]\n"
    "ds_read_b128 v[188:191], v9 offset:8192\n"
    "buffer_load_dwordx4 v17, s[12:15], s74 offen offset:3072 lds\n"
    "v_cndmask_b32 v37, 0, v37, vcc\n"
    "v_subrev_u32 v56, 6, v60\n"
    "v_cmp_le_i32 vcc, 0, v56\n"
    "v_mfma_f32_32x32x16_bf16 v[80:95], v[148:151], v[152:155], v[80:95]\n"
    "ds_read_b128 v[180:183], v5 offset:4096\n"
    "v_lshl_add_u32 v56, v56, 9, v57\n"
    "v_cndmask_b32 v51, v58, v56, vcc\n"
    "v_cndmask_b32 v38, 0, v38, vcc\n"
    "v_cndmask_b32 v39, 0, v39, vcc\n"
    "v_subrev_u32 v56, 8, v60\n"
    "v_cmp_le_i32 vcc, 0, v56\n"
    "s_waitcnt vmcnt(8)\n"
    "s_barrier\n"
    "s_waitcnt lgkmcnt(4)\n"
    "v_mfma_f32_32x32x16_bf16 v[64:79], v[160:163], v[168:171], v[64:79]\n"
    "s_add_u32 m0, s77, 98304\n"
    "s_xor_b32 s74, s18, 768\n"
    "ds_read_b128 v[128:131], v18 offset:0\n"
    "buffer_load_dwordx4 v10, s[8:11], s74 offen offset:0 lds\n"
    "v_lshl_add_u32 v56, v56, 9, v57\n"
    "v_cndmask_b32 v52, v58, v56, vcc\n"
    "v_mfma_f32_32x32x16_bf16 v[96:111], v[160:163], v[172:175], v[96:111]\n"
    "ds_read_b128 v[136:139], v22 offset:0\n"
    "v_cndmask_b32 v40, 0, v40, vcc\n"
    "v_cndmask_b32 v41, 0, v41, vcc\n"
    "v_subrev_u32 v56, 10, v60\n"
    "v_cmp_le_i32 vcc, 0, v56\n"
    "v_lshl_add_u32 v56, v56, 9, v57\n"
    "s_nop 0\n"
    "v_cndmask_b32 v53, v58, v56, vcc\n"
    "v_mfma_f32_32x32x16_bf16 v[112:127], v[164:167], v[172:175], v[112:127]\n"
    "ds_read_b128 v[140:143], v22 offset:8192\n"
    "buffer_load_dwordx4 v11, s[8:11], s74 offen offset:1024 lds\n"
    "v_cndmask_b32 v42, 0, v42, vcc\n"
    "v_cndmask_b32 v43, 0, v43, vcc\n"
    "v_subrev_u32 v56, 12, v60\n"
    "v_mfma_f32_32x32x16_bf16 v[80:95], v[164:167], v[168:171], v[80:95]\n"
    "ds_read_b128 v[132:135], v18 offset:4096\n"
    "v_cmp_le_i32 vcc, 0, v56\n"
    "v_lshl_add_u32 v56, v56, 9, v57\n"
    "s_nop 0\n"
    "v_cndmask_b32 v54, v58, v56, vcc\n"
    "v_cndmask_b32 v44, 0, v44, vcc\n"
    "v_cndmask_b32 v45, 0, v45, vcc\n"
    "v_subrev_u32 v56, 14, v60\n"
    "s_waitcnt lgkmcnt(4)\n"
    "v_mfma_f32_32x32x16_bf16 v[64:79], v[176:179], v[184:187], v[64:79]\n"
    "ds_read_b128 v[144:147], v19 offset:0\n"
    "buffer_load_dwordx4 v12, s[8:11], s74 offen offset:2048 lds\n"
    "v_cmp_le_i32 vcc, 0, v56\n"
    "v_lshl_add_u32 v56, v56, 9, v57\n"
    "v_mfma_f32_32x32x16_bf16 v[96:111], v[176:179], v[188:191], v[96:111]\n"
    "ds_read_b128 v[152:155], v23 offset:0\n"
    "v_cndmask_b32 v55, v58, v56, vcc\n"
    "v_cndmask_b32 v46, 0, v46, vcc\n"
    "v_cndmask_b32 v47, 0, v47, vcc\n"
    "s_lshl_b32 s4, s78, 13\n"
    "s_add_u32 s4, s4, 65536\n"
    "s_add_u32 s28, s46, s4\n"
    "s_addc_u32 s29, s47, 0\n"
    "v_mfma_f32_32x32x16_bf16 v[112:127], v[180:183], v[188:191], v[112:127]\n"
    "ds_read_b128 v[156:159], v23 offset:8192\n"
    "buffer_load_dwordx4 v13, s[8:11], s74 offen offset:3072 lds\n"
    "s_and_b32 s29, s29, 0xffff\n"
    "s_mov_b32 s30, 8192\n"
    "s_mov_b32 s31, 0x20000\n"
    "v_mfma_f32_32x32x16_bf16 v[80:95], v[180:183], v[184:187], v[80:95]\n"
    "ds_read_b128 v[148:151], v19 offset:4096\n"
    "s_lshl_b32 s4, s67, 10\n"
    "s_add_u32 s4, s4, 131072\n"
    "s_lshl_b32 s5, s66, 9\n"
    "s_add_u32 s5, s5, s4\n"
    "v_lshl_add_u32 v56, v1, 3, s5\n"
    "v_lshl_add_u32 v57, v61, 3, s4\n"
    "s_waitcnt lgkmcnt(4)\n"
    "v_mfma_f32_32x32x16_bf16 v[64:79], v[128:131], v[136:139], v[64:79]\n"
    "s_add_u32 m0, s77, 114688\n"
    "ds_read_b128 v[160:163], v20 offset:0\n"
    "buffer_load_dwordx4 v14, s[12:15], s74 offen offset:0 lds\n"
    "s_lshl_b32 s4, s68, 18\n"
    "v_mfma_f32_32x32x16_bf16 v[96:111], v[128:131], v[140:143], v[96:111]\n"
    "ds_read_b128 v[168:171], v24 offset:0\n"
    "s_lshl_b32 s5, s66, 16\n"
    "s_add_u32 s4, s4, s5\n"
    "v_mfma_f32_32x32x16_bf16 v[112:127], v[132:135], v[140:143], v[112:127]\n"
    "ds_read_b128 v[172:175], v24 offset:8192\n"
    "buffer_load_dwordx4 v15, s[12:15], s74 offen offset:1024 lds\n"
    "s_lshl_b32 s6, s78, 12\n"
    "v_mfma_f32_32x32x16_bf16 v[80:95], v[132:135], v[136:139], v[80:95]\n"
    "ds_read_b128 v[164:167], v20 offset:4096\n"
    "s_add_u32 s4, s4, s6\n"
    "s_add_u32 s4, s4, 4194304\n"
    "s_waitcnt lgkmcnt(4)\n"
    "v_mfma_f32_32x32x16_bf16 v[64:79], v[144:147], v[152:155], v[64:79]\n"
    "ds_read_b128 v[176:179], v21 offset:0\n"
    "buffer_load_dwordx4 v16, s[12:15], s74 offen offset:2048 lds\n"
    "v_lshrrev_b32 v59, 5, v1\n"
    "v_mfma_f32_32x32x16_bf16 v[96:111], v[144:147], v[156:159], v[96:111]\n"
    "ds_read_b128 v[184:187], v25 offset:0\n"
    "v_lshlrev_b32 v59, 3, v59\n"
    "v_xor_b32 v59, v59, v1\n"
    "v_mfma_f32_32x32x16_bf16 v[112:127], v[148:151], v[156:159], v[112:127]\n"
    "ds_read_b128 v[188:191], v25 offset:8192\n"
    "buffer_load_dwordx4 v17, s[12:15], s74 offen offset:3072 lds\n"
    "v_lshl_add_u32 v59, v59, 4, s4\n"
    "v_mfma_f32_32x32x16_bf16 v[80:95], v[148:151], v[152:155], v[80:95]\n"
    "ds_read_b128 v[180:183], v21 offset:4096\n"
    "s_lshl_b32 s4, s68, 3\n"
    "s_add_u32 s4, s4, s69\n"
    "s_lshl_b32 s4, s4, 7\n"
    "s_waitcnt vmcnt(8)\n"
    "s_barrier\n"
    "s_waitcnt lgkmcnt(4)\n"
    "v_mfma_f32_32x32x16_bf16 v[64:79], v[160:163], v[168:171], v[64:79]\n"
    "s_add_u32 m0, s77, 65536\n"
    "s_xor_b32 s74, s18, 896\n"
    "ds_read_b128 v[128:131], v18 offset:32768\n"
    "buffer_load_dwordx4 v10, s[8:11], s74 offen offset:0 lds\n"
    "s_add_u32 s4, s4, 655360\n"
    "v_mfma_f32_32x32x16_bf16 v[96:111], v[160:163], v[172:175], v[96:111]\n"
    "ds_read_b128 v[136:139], v22 offset:32768\n"
    "s_cmp_eq_u32 s65, 0\n"
    "s_cselect_b32 s16, s4, 2146435072\n"
    "s_add_u32 s17, s16, 16384\n"
    "v_mfma_f32_32x32x16_bf16 v[112:127], v[164:167], v[172:175], v[112:127]\n"
    "ds_read_b128 v[140:143], v22 offset:40960\n"
    "buffer_load_dwordx4 v11, s[8:11], s74 offen offset:1024 lds\n"
    "s_and_b32 s87, s69, 1\n"
    "v_mfma_f32_32x32x16_bf16 v[80:95], v[164:167], v[168:171], v[80:95]\n"
    "ds_read_b128 v[132:135], v18 offset:36864\n"
    "s_lshl_b32 s4, s71, 1\n"
    "s_or_b32 s87, s87, s4\n"
    "s_xor_b32 s4, s87, 0\n"
    "s_waitcnt lgkmcnt(4)\n"
    "v_mfma_f32_32x32x16_bf16 v[64:79], v[176:179], v[184:187], v[64:79]\n"
    "ds_read_b128 v[144:147], v19 offset:32768\n"
    "buffer_load_dwordx4 v12, s[8:11], s74 offen offset:2048 lds\n"
    "s_lshr_b32 s5, s4, 1\n"
    "v_mfma_f32_32x32x16_bf16 v[96:111], v[176:179], v[188:191], v[96:111]\n"
    "ds_read_b128 v[152:155], v23 offset:32768\n"
    "s_lshl_b32 s5, s5, 8\n"
    "s_bitcmp1_b32 s4, 0\n"
    "s_cselect_b32 s24, s60, s58\n"
    "s_cselect_b32 s25, s61, s59\n"
    "v_mfma_f32_32x32x16_bf16 v[112:127], v[180:183], v[188:191], v[112:127]\n"
    "ds_read_b128 v[156:159], v23 offset:40960\n"
    "buffer_load_dwordx4 v13, s[8:11], s74 offen offset:3072 lds\n"
    "s_add_u32 s24, s24, s5\n"
    "s_addc_u32 s25, s25, 0\n"
    "v_mfma_f32_32x32x16_bf16 v[80:95], v[180:183], v[184:187], v[80:95]\n"
    "ds_read_b128 v[148:151], v19 offset:36864\n"
    "s_and_b32 s25, s25, 0xffff\n"
    "s_sub_u32 s26, 524288, s5\n"
    "s_mov_b32 s27, 0x20000\n"
    "s_waitcnt lgkmcnt(4)\n"
    "v_mfma_f32_32x32x16_bf16 v[64:79], v[128:131], v[136:139], v[64:79]\n"
    "s_add_u32 m0, s77, 81920\n"
    "ds_read_b128 v[160:163], v20 offset:32768\n"
    "buffer_load_dwordx4 v14, s[12:15], s74 offen offset:0 lds\n"
    "v_mfma_f32_32x32x16_bf16 v[96:111], v[128:131], v[140:143], v[96:111]\n"
    "ds_read_b128 v[168:171], v24 offset:32768\n"
    "v_mfma_f32_32x32x16_bf16 v[112:127], v[132:135], v[140:143], v[112:127]\n"
    "ds_read_b128 v[172:175], v24 offset:40960\n"
    "buffer_load_dwordx4 v15, s[12:15], s74 offen offset:1024 lds\n"
    "v_mfma_f32_32x32x16_bf16 v[80:95], v[132:135], v[136:139], v[80:95]\n"
    "ds_read_b128 v[164:167], v20 offset:36864\n"
    "s_waitcnt lgkmcnt(4)\n"
    "v_mfma_f32_32x32x16_bf16 v[64:79], v[144:147], v[152:155], v[64:79]\n"
    "ds_read_b128 v[176:179], v21 offset:32768\n"
    "buffer_load_dwordx4 v16, s[12:15], s74 offen offset:2048 lds\n"
    "v_mfma_f32_32x32x16_bf16 v[96:111], v[144:147], v[156:159], v[96:111]\n"
    "ds_read_b128 v[184:187], v25 offset:32768\n"
    "v_mfma_f32_32x32x16_bf16 v[112:127], v[148:151], v[156:159], v[112:127]\n"
    "ds_read_b128 v[188:191], v25 offset:40960\n"
    "buffer_load_dwordx4 v17, s[12:15], s74 offen offset:3072 lds\n"
    "v_mfma_f32_32x32x16_bf16 v[80:95], v[148:151], v[152:155], v[80:95]\n"
    "ds_read_b128 v[180:183], v21 offset:36864\n"
    "v_lshrrev_b32 v10, 4, v1\n"
    "v_xor_b32 v11, v10, v1\n"
    "v_and_b32 v11, 15, v11\n"
    "v_lshlrev_b32 v11, 4, v11\n"
    "v_lshl_or_b32 v10, v10, 10, v11\n"
    "s_lshr_b32 s4, s69, 1\n"
    "s_lshl_b32 s4, s4, 3\n"
    "s_add_u32 s4, s4, s65\n"
    "s_lshl_b32 s4, s4, 14\n"
    "v_add_u32 v10, s4, v10\n"
    "v_xor_b32 v11, 64, v10\n"
    "v_add_u32 v11, 3072, v11\n"
    "v_xor_b32 v12, 128, v10\n"
    "v_add_u32 v12, 6144, v12\n"
    "v_xor_b32 v13, 192, v10\n"
    "v_add_u32 v13, 9216, v13\n"
    "s_waitcnt vmcnt(8)\n"
    "s_barrier\n"
    "s_waitcnt lgkmcnt(4)\n"
    "v_mfma_f32_32x32x16_bf16 v[64:79], v[160:163], v[168:171], v[64:79]\n"
    "s_mov_b32 m0, s77\n"
    "ds_read_b128 v[128:131], v2 offset:32768\n"
    "buffer_load_dwordx4 v10, s[24:27], 0 offen offset:0 lds\n"
    "v_mfma_f32_32x32x16_bf16 v[96:111], v[160:163], v[172:175], v[96:111]\n"
    "ds_read_b128 v[136:139], v6 offset:32768\n"
    "v_mfma_f32_32x32x16_bf16 v[112:127], v[164:167], v[172:175], v[112:127]\n"
    "ds_read_b128 v[140:143], v6 offset:40960\n"
    "buffer_load_dwordx4 v11, s[24:27], 0 offen offset:1024 lds\n"
    "v_mfma_f32_32x32x16_bf16 v[80:95], v[164:167], v[168:171], v[80:95]\n"
    "ds_read_b128 v[132:135], v2 offset:36864\n"
    "s_waitcnt lgkmcnt(4)\n"
    "v_mfma_f32_32x32x16_bf16 v[64:79], v[176:179], v[184:187], v[64:79]\n"
    "ds_read_b128 v[144:147], v3 offset:32768\n"
    "buffer_load_dwordx4 v12, s[24:27], 0 offen offset:2048 lds\n"
    "v_mfma_f32_32x32x16_bf16 v[96:111], v[176:179], v[188:191], v[96:111]\n"
    "ds_read_b128 v[152:155], v7 offset:32768\n"
    "v_mfma_f32_32x32x16_bf16 v[112:127], v[180:183], v[188:191], v[112:127]\n"
    "ds_read_b128 v[156:159], v7 offset:40960\n"
    "buffer_load_dwordx4 v13, s[24:27], 0 offen offset:3072 lds\n"
    "v_mfma_f32_32x32x16_bf16 v[80:95], v[180:183], v[184:187], v[80:95]\n"
    "ds_read_b128 v[148:151], v3 offset:36864\n"
    "s_waitcnt lgkmcnt(4)\n"
    "v_mfma_f32_32x32x16_bf16 v[64:79], v[128:131], v[136:139], v[64:79]\n"
    "s_add_u32 m0, s77, 16384\n"
    "s_mov_b32 s74, 65536\n"
    "ds_read_b128 v[160:163], v4 offset:32768\n"
    "buffer_load_dwordx4 v10, s[24:27], s74 offen offset:0 lds\n"
    "v_mfma_f32_32x32x16_bf16 v[96:111], v[128:131], v[140:143], v[96:111]\n"
    "ds_read_b128 v[168:171], v8 offset:32768\n"
    "v_mfma_f32_32x32x16_bf16 v[112:127], v[132:135], v[140:143], v[112:127]\n"
    "ds_read_b128 v[172:175], v8 offset:40960\n"
    "buffer_load_dwordx4 v11, s[24:27], s74 offen offset:1024 lds\n"
    "v_mfma_f32_32x32x16_bf16 v[80:95], v[132:135], v[136:139], v[80:95]\n"
    "ds_read_b128 v[164:167], v4 offset:36864\n"
    "s_waitcnt lgkmcnt(4)\n"
    "v_mfma_f32_32x32x16_bf16 v[64:79], v[144:147], v[152:155], v[64:79]\n"
    "ds_read_b128 v[176:179], v5 offset:32768\n"
    "buffer_load_dwordx4 v12, s[24:27], s74 offen offset:2048 lds\n"
    "v_mfma_f32_32x32x16_bf16 v[96:111], v[144:147], v[156:159], v[96:111]\n"
    "ds_read_b128 v[184:187], v9 offset:32768\n"
    "v_mfma_f32_32x32x16_bf16 v[112:127], v[148:151], v[156:159], v[112:127]\n"
    "ds_read_b128 v[188:191], v9 offset:40960\n"
    "buffer_load_dwordx4 v13, s[24:27], s74 offen offset:3072 lds\n"
    "v_mfma_f32_32x32x16_bf16 v[80:95], v[148:151], v[152:155], v[80:95]\n"
    "ds_read_b128 v[180:183], v5 offset:36864\n"
    "s_waitcnt vmcnt(8)\n"
    "s_barrier\n"
    "s_waitcnt lgkmcnt(4)\n"
    "v_mfma_f32_32x32x16_bf16 v[64:79], v[160:163], v[168:171], v[64:79]\n"
    "s_xor_b32 s84, s87, 1\n"
    "s_lshr_b32 s85, s84, 1\n"
    "s_lshl_b32 s85, s85, 8\n"
    "s_bitcmp1_b32 s84, 0\n"
    "s_cselect_b32 s12, s60, s58\n"
    "s_cselect_b32 s13, s61, s59\n"
    "s_add_u32 s12, s12, s85\n"
    "s_addc_u32 s13, s13, 0\n"
    "s_and_b32 s13, s13, 0xffff\n"
    "s_sub_u32 s14, 524288, s85\n"
    "s_add_u32 m0, s77, 32768\n"
    "s_nop 0\n"
    "ds_read_b128 v[128:131], v2 offset:0\n"
    "buffer_load_dwordx4 v10, s[12:15], 0 offen offset:0 lds\n"
    "v_mfma_f32_32x32x16_bf16 v[96:111], v[160:163], v[172:175], v[96:111]\n"
    "ds_read_b128 v[136:139], v6 offset:0\n"
    "v_mfma_f32_32x32x16_bf16 v[112:127], v[164:167], v[172:175], v[112:127]\n"
    "ds_read_b128 v[140:143], v6 offset:8192\n"
    "buffer_load_dwordx4 v11, s[12:15], 0 offen offset:1024 lds\n"
    "v_mfma_f32_32x32x16_bf16 v[80:95], v[164:167], v[168:171], v[80:95]\n"
    "ds_read_b128 v[132:135], v2 offset:4096\n"
    "s_waitcnt lgkmcnt(4)\n"
    "v_mfma_f32_32x32x16_bf16 v[64:79], v[176:179], v[184:187], v[64:79]\n"
    "ds_read_b128 v[144:147], v3 offset:0\n"
    "buffer_load_dwordx4 v12, s[12:15], 0 offen offset:2048 lds\n"
    "v_mfma_f32_32x32x16_bf16 v[96:111], v[176:179], v[188:191], v[96:111]\n"
    "ds_read_b128 v[152:155], v7 offset:0\n"
    "v_mfma_f32_32x32x16_bf16 v[112:127], v[180:183], v[188:191], v[112:127]\n"
    "ds_read_b128 v[156:159], v7 offset:8192\n"
    "buffer_load_dwordx4 v13, s[12:15], 0 offen offset:3072 lds\n"
    "v_mfma_f32_32x32x16_bf16 v[80:95], v[180:183], v[184:187], v[80:95]\n"
    "ds_read_b128 v[148:151], v3 offset:4096\n"
    "s_waitcnt lgkmcnt(4)\n"
    "v_mfma_f32_32x32x16_bf16 v[64:79], v[128:131], v[136:139], v[64:79]\n"
    "s_add_u32 m0, s77, 49152\n"
    "s_nop 0\n"
    "ds_read_b128 v[160:163], v4 offset:0\n"
    "buffer_load_dwordx4 v10, s[12:15], s74 offen offset:0 lds\n"
    "v_mfma_f32_32x32x16_bf16 v[96:111], v[128:131], v[140:143], v[96:111]\n"
    "ds_read_b128 v[168:171], v8 offset:0\n"
    "v_mfma_f32_32x32x16_bf16 v[112:127], v[132:135], v[140:143], v[112:127]\n"
    "ds_read_b128 v[172:175], v8 offset:8192\n"
    "buffer_load_dwordx4 v11, s[12:15], s74 offen offset:1024 lds\n"
    "v_mfma_f32_32x32x16_bf16 v[80:95], v[132:135], v[136:139], v[80:95]\n"
    "ds_read_b128 v[164:167], v4 offset:4096\n"
    "s_waitcnt lgkmcnt(4)\n"
    "v_mfma_f32_32x32x16_bf16 v[64:79], v[144:147], v[152:155], v[64:79]\n"
    "ds_read_b128 v[176:179], v5 offset:0\n"
    "buffer_load_dwordx4 v12, s[12:15], s74 offen offset:2048 lds\n"
    "v_mfma_f32_32x32x16_bf16 v[96:111], v[144:147], v[156:159], v[96:111]\n"
    "ds_read_b128 v[184:187], v9 offset:0\n"
    "v_mfma_f32_32x32x16_bf16 v[112:127], v[148:151], v[156:159], v[112:127]\n"
    "ds_read_b128 v[188:191], v9 offset:8192\n"
    "buffer_load_dwordx4 v13, s[12:15], s74 offen offset:3072 lds\n"
    "v_mfma_f32_32x32x16_bf16 v[80:95], v[148:151], v[152:155], v[80:95]\n"
    "ds_read_b128 v[180:183], v5 offset:4096\n"
    "s_waitcnt lgkmcnt(4)\n"
    "v_mfma_f32_32x32x16_bf16 v[64:79], v[160:163], v[168:171], v[64:79]\n"
    "v_mfma_f32_32x32x16_bf16 v[96:111], v[160:163], v[172:175], v[96:111]\n"
    "s_waitcnt lgkmcnt(0)\n"
    "v_mfma_f32_32x32x16_bf16 v[64:79], v[176:179], v[184:187], v[64:79]\n"
    "v_mfma_f32_32x32x16_bf16 v[96:111], v[176:179], v[188:191], v[96:111]\n"
    "v_mfma_f32_32x32x16_bf16 v[112:127], v[164:167], v[172:175], v[112:127]\n"
    "v_mfma_f32_32x32x16_bf16 v[80:95], v[164:167], v[168:171], v[80:95]\n"
    "v_mfma_f32_32x32x16_bf16 v[112:127], v[180:183], v[188:191], v[112:127]\n"
    "v_mfma_f32_32x32x16_bf16 v[80:95], v[180:183], v[184:187], v[80:95]\n"
    "s_nop 6\n"
    "v_pk_mul_f32 v[2:3], v[222:223], v[64:65] op_sel:[1,0] op_sel_hi:[0,1]\n"
    "v_pk_mul_f32 v[4:5], v[254:255], v[96:97] op_sel:[1,0] op_sel_hi:[0,1]\n"
    "v_pk_mul_f32 v[6:7], v[222:223], v[96:97] op_sel:[1,0] op_sel_hi:[0,1]\n"
    "v_pk_mul_f32 v[8:9], v[254:255], v[64:65] op_sel:[1,0] op_sel_hi:[0,1]\n"
    "v_pk_fma_f32 v[2:3], v[220:221], v[66:67], v[2:3] op_sel:[1,0,0] op_sel_hi:[0,1,1]\n"
    "v_pk_fma_f32 v[4:5], v[252:253], v[98:99], v[4:5] op_sel:[1,0,0] op_sel_hi:[0,1,1]\n"
    "v_pk_fma_f32 v[6:7], v[220:221], v[98:99], v[6:7] op_sel:[1,0,0] op_sel_hi:[0,1,1]\n"
    "v_pk_fma_f32 v[8:9], v[252:253], v[66:67], v[8:9] op_sel:[1,0,0] op_sel_hi:[0,1,1]\n"
    "v_pk_fma_f32 v[2:3], v[218:219], v[68:69], v[2:3] op_sel:[1,0,0] op_sel_hi:[0,1,1]\n"
    "v_pk_fma_f32 v[4:5], v[250:251], v[100:101], v[4:5] op_sel:[1,0,0] op_sel_hi:[0,1,1]\n"
    "v_pk_fma_f32 v[6:7], v[218:219], v[100:101], v[6:7] op_sel:[1,0,0] op_sel_hi:[0,1,1]\n"
    "v_pk_fma_f32 v[8:9], v[250:251], v[68:69], v[8:9] op_sel:[1,0,0] op_sel_hi:[0,1,1]\n"
    "v_pk_fma_f32 v[2:3], v[216:217], v[70:71], v[2:3] op_sel:[1,0,0] op_sel_hi:[0,1,1]\n"
    "v_pk_fma_f32 v[4:5], v[248:249], v[102:103], v[4:5] op_sel:[1,0,0] op_sel_hi:[0,1,1]\n"
    "v_pk_fma_f32 v[6:7], v[216:217], v[102:103], v[6:7] op_sel:[1,0,0] op_sel_hi:[0,1,1]\n"
    "v_pk_fma_f32 v[8:9], v[248:249], v[70:71], v[8:9] op_sel:[1,0,0] op_sel_hi:[0,1,1]\n"
    "v_pk_fma_f32 v[2:3], v[214:215], v[72:73], v[2:3] op_sel:[1,0,0] op_sel_hi:[0,1,1]\n"
    "v_pk_fma_f32 v[4:5], v[246:247], v[104:105], v[4:5] op_sel:[1,0,0] op_sel_hi:[0,1,1]\n"
    "v_pk_fma_f32 v[6:7], v[214:215], v[104:105], v[6:7] op_sel:[1,0,0] op_sel_hi:[0,1,1]\n"
    "v_pk_fma_f32 v[8:9], v[246:247], v[72:73], v[8:9] op_sel:[1,0,0] op_sel_hi:[0,1,1]\n"
    "v_pk_fma_f32 v[2:3], v[212:213], v[74:75], v[2:3] op_sel:[1,0,0] op_sel_hi:[0,1,1]\n"
    "v_pk_fma_f32 v[4:5], v[244:245], v[106:107], v[4:5] op_sel:[1,0,0] op_sel_hi:[0,1,1]\n"
    "v_pk_fma_f32 v[6:7], v[212:213], v[106:107], v[6:7] op_sel:[1,0,0] op_sel_hi:[0,1,1]\n"
    "v_pk_fma_f32 v[8:9], v[244:245], v[74:75], v[8:9] op_sel:[1,0,0] op_sel_hi:[0,1,1]\n"
    "v_pk_fma_f32 v[2:3], v[210:211], v[76:77], v[2:3] op_sel:[1,0,0] op_sel_hi:[0,1,1]\n"
    "v_pk_fma_f32 v[4:5], v[242:243], v[108:109], v[4:5] op_sel:[1,0,0] op_sel_hi:[0,1,1]\n"
    "v_pk_fma_f32 v[6:7], v[210:211], v[108:109], v[6:7] op_sel:[1,0,0] op_sel_hi:[0,1,1]\n"
    "v_pk_fma_f32 v[8:9], v[242:243], v[76:77], v[8:9] op_sel:[1,0,0] op_sel_hi:[0,1,1]\n"
    "v_pk_fma_f32 v[2:3], v[208:209], v[78:79], v[2:3] op_sel:[1,0,0] op_sel_hi:[0,1,1]\n"
    "v_pk_fma_f32 v[4:5], v[240:241], v[110:111], v[4:5] op_sel:[1,0,0] op_sel_hi:[0,1,1]\n"
    "v_pk_fma_f32 v[6:7], v[208:209], v[110:111], v[6:7] op_sel:[1,0,0] op_sel_hi:[0,1,1]\n"
    "v_pk_fma_f32 v[8:9], v[240:241], v[78:79], v[8:9] op_sel:[1,0,0] op_sel_hi:[0,1,1]\n"
    "v_pk_fma_f32 v[2:3], v[206:207], v[80:81], v[2:3] op_sel:[1,0,0] op_sel_hi:[0,1,1]\n"
    "v_pk_fma_f32 v[4:5], v[238:239], v[112:113], v[4:5] op_sel:[1,0,0] op_sel_hi:[0,1,1]\n"
    "v_pk_fma_f32 v[6:7], v[206:207], v[112:113], v[6:7] op_sel:[1,0,0] op_sel_hi:[0,1,1]\n"
    "v_pk_fma_f32 v[8:9], v[238:239], v[80:81], v[8:9] op_sel:[1,0,0] op_sel_hi:[0,1,1]\n"
    "v_pk_fma_f32 v[2:3], v[204:205], v[82:83], v[2:3] op_sel:[1,0,0] op_sel_hi:[0,1,1]\n"
    "v_pk_fma_f32 v[4:5], v[236:237], v[114:115], v[4:5] op_sel:[1,0,0] op_sel_hi:[0,1,1]\n"
    "v_pk_fma_f32 v[6:7], v[204:205], v[114:115], v[6:7] op_sel:[1,0,0] op_sel_hi:[0,1,1]\n"
    "v_pk_fma_f32 v[8:9], v[236:237], v[82:83], v[8:9] op_sel:[1,0,0] op_sel_hi:[0,1,1]\n"
    "v_pk_fma_f32 v[2:3], v[202:203], v[84:85], v[2:3] op_sel:[1,0,0] op_sel_hi:[0,1,1]\n"
    "v_pk_fma_f32 v[4:5], v[234:235], v[116:117], v[4:5] op_sel:[1,0,0] op_sel_hi:[0,1,1]\n"
    "v_pk_fma_f32 v[6:7], v[202:203], v[116:117], v[6:7] op_sel:[1,0,0] op_sel_hi:[0,1,1]\n"
    "v_pk_fma_f32 v[8:9], v[234:235], v[84:85], v[8:9] op_sel:[1,0,0] op_sel_hi:[0,1,1]\n"
    "v_pk_fma_f32 v[2:3], v[200:201], v[86:87], v[2:3] op_sel:[1,0,0] op_sel_hi:[0,1,1]\n"
    "v_pk_fma_f32 v[4:5], v[232:233], v[118:119], v[4:5] op_sel:[1,0,0] op_sel_hi:[0,1,1]\n"
    "v_pk_fma_f32 v[6:7], v[200:201], v[118:119], v[6:7] op_sel:[1,0,0] op_sel_hi:[0,1,1]\n"
    "v_pk_fma_f32 v[8:9], v[232:233], v[86:87], v[8:9] op_sel:[1,0,0] op_sel_hi:[0,1,1]\n"
    "v_pk_fma_f32 v[2:3], v[198:199], v[88:89], v[2:3] op_sel:[1,0,0] op_sel_hi:[0,1,1]\n"
    "v_pk_fma_f32 v[4:5], v[230:231], v[120:121], v[4:5] op_sel:[1,0,0] op_sel_hi:[0,1,1]\n"
    "v_pk_fma_f32 v[6:7], v[198:199], v[120:121], v[6:7] op_sel:[1,0,0] op_sel_hi:[0,1,1]\n"
    "v_pk_fma_f32 v[8:9], v[230:231], v[88:89], v[8:9] op_sel:[1,0,0] op_sel_hi:[0,1,1]\n"
    "v_pk_fma_f32 v[2:3], v[196:197], v[90:91], v[2:3] op_sel:[1,0,0] op_sel_hi:[0,1,1]\n"
    "v_pk_fma_f32 v[4:5], v[228:229], v[122:123], v[4:5] op_sel:[1,0,0] op_sel_hi:[0,1,1]\n"
    "v_pk_fma_f32 v[6:7], v[196:197], v[122:123], v[6:7] op_sel:[1,0,0] op_sel_hi:[0,1,1]\n"
    "v_pk_fma_f32 v[8:9], v[228:229], v[90:91], v[8:9] op_sel:[1,0,0] op_sel_hi:[0,1,1]\n"
    "v_pk_fma_f32 v[2:3], v[194:195], v[92:93], v[2:3] op_sel:[1,0,0] op_sel_hi:[0,1,1]\n"
    "v_pk_fma_f32 v[4:5], v[226:227], v[124:125], v[4:5] op_sel:[1,0,0] op_sel_hi:[0,1,1]\n"
    "v_pk_fma_f32 v[6:7], v[194:195], v[124:125], v[6:7] op_sel:[1,0,0] op_sel_hi:[0,1,1]\n"
    "v_pk_fma_f32 v[8:9], v[226:227], v[92:93], v[8:9] op_sel:[1,0,0] op_sel_hi:[0,1,1]\n"
    "v_pk_fma_f32 v[2:3], v[192:193], v[94:95], v[2:3] op_sel:[1,0,0] op_sel_hi:[0,1,1]\n"
    "v_pk_fma_f32 v[4:5], v[224:225], v[126:127], v[4:5] op_sel:[1,0,0] op_sel_hi:[0,1,1]\n"
    "v_pk_fma_f32 v[6:7], v[192:193], v[126:127], v[6:7] op_sel:[1,0,0] op_sel_hi:[0,1,1]\n"
    "v_pk_fma_f32 v[8:9], v[224:225], v[94:95], v[8:9] op_sel:[1,0,0] op_sel_hi:[0,1,1]\n"
    "v_pk_add_f32 v[2:3], v[2:3], v[4:5] neg_lo:[0,1] neg_hi:[0,1]\n"
    "v_pk_add_f32 v[6:7], v[6:7], v[8:9]\n"
    "v_add_f32 v2, v2, v3\n"
    "v_add_f32 v3, v6, v7\n"
    "ds_write_b64 v56, v[2:3]\n"
    "v_mov_b32 v141, s80\n"
    "v_mov_b32 v143, s81\n"
    "s_waitcnt lgkmcnt(0)\n"
    "s_barrier\n"
    "ds_read_b64 v[128:129], v57 offset:0\n"
    "ds_read_b64 v[130:131], v57 offset:256\n"
    "ds_read_b64 v[132:133], v57 offset:512\n"
    "ds_read_b64 v[134:135], v57 offset:768\n"
    "s_waitcnt lgkmcnt(2)\n"
    "v_fma_f32 v136, v128, v30, v130\n"
    "v_fma_f32 v137, v128, v31, v131\n"
    "v_fma_f32 v136, -v129, v31, v136\n"
    "v_fmac_f32 v137, v129, v30\n"
    "s_waitcnt lgkmcnt(0)\n"
    "v_fma_f32 v138, v136, v30, v132\n"
    "v_fma_f32 v139, v136, v31, v133\n"
    "v_fma_f32 v138, -v137, v31, v138\n"
    "v_fmac_f32 v139, v137, v30\n"
    "v_fma_f32 v140, v138, v30, v134\n"
    "v_fma_f32 v142, v138, v31, v135\n"
    "v_fma_f32 v140, -v139, v31, v140\n"
    "v_fmac_f32 v142, v139, v30\n"
    "s_cmp_lg_u32 s66, 0\n"
    "s_cbranch_scc1 Lnp_2%=\n"
    "buffer_store_dwordx4 v[140:143], v58, s[28:31], 0 offen sc1\n"
    "Lnp_2%=:\n"
    "s_and_b32 s4, s69, 1\n"
    "s_lshl_b32 s4, s4, 2\n"
    "s_add_u32 s4, s4, s67\n"
    "s_lshl_b32 s4, s4, 14\n"
    "s_and_b32 s5, s69, 6\n"
    "s_add_u32 s5, s5, s66\n"
    "s_lshl_b32 s5, s5, 7\n"
    "s_add_u32 s4, s4, s5\n"
    "v_lshrrev_b32 v18, 4, v1\n"
    "s_xor_b32 s84, s87, 2\n"
    "s_lshr_b32 s85, s84, 1\n"
    "s_lshl_b32 s85, s85, 8\n"
    "s_bitcmp1_b32 s84, 0\n"
    "s_cselect_b32 s12, s60, s58\n"
    "s_cselect_b32 s13, s61, s59\n"
    "s_add_u32 s12, s12, s85\n"
    "s_addc_u32 s13, s13, 0\n"
    "s_and_b32 s13, s13, 0xffff\n"
    "s_sub_u32 s14, 524288, s85\n"
    "s_add_u32 m0, s77, 65536\n"
    "s_mov_b32 s86, 65536\n"
    "buffer_load_dwordx4 v10, s[12:15], 0 offen lds\n"
    "v_xor_b32 v18, v18, v1\n"
    "v_and_b32 v18, 7, v18\n"
    "v_lshlrev_b32 v18, 4, v18\n"
    "v_lshrrev_b32 v19, 3, v1\n"
    "v_lshl_or_b32 v18, v19, 10, v18\n"
    "v_add_u32 v18, s4, v18\n"
    "buffer_load_dwordx4 v11, s[12:15], 0 offen offset:1024 lds\n"
    "v_xor_b32 v19, 64, v18\n"
    "v_add_u32 v20, 30720, v18\n"
    "v_add_u32 v21, 37888, v19\n"
    "v_add_u32 v19, 7168, v19\n"
    "s_lshl_b32 s4, s68, 17\n"
    "s_add_u32 s8, s40, s4\n"
    "s_addc_u32 s9, s41, 0\n"
    "s_and_b32 s9, s9, 0xffff\n"
    "s_mov_b32 s10, 131072\n"
    "s_mov_b32 s11, 0x20000\n"
    "v_mul_f32 v6, v31, v31\n"
    "buffer_load_dwordx4 v12, s[12:15], 0 offen offset:2048 lds\n"
    "v_mul_f32 v7, v30, v31\n"
    "v_fma_f32 v6, v30, v30, -v6\n"
    "v_add_f32 v7, v7, v7\n"
    "v_mul_f32 v8, v6, v30\n"
    "v_mul_f32 v9, v6, v31\n"
    "v_fma_f32 v8, -v7, v31, v8\n"
    "v_fmac_f32 v9, v7, v30\n"
    "s_cmp_lg_u32 s66, 0\n"
    "s_cselect_b64 s[82:83], -1, 0\n"
    "v_cmp_lt_u32 vcc, 31, v1\n"
    "v_mov_b32 v22, 1.0\n"
    "buffer_load_dwordx4 v13, s[12:15], 0 offen offset:3072 lds\n"
    "v_mov_b32 v23, 0\n"
    "v_cndmask_b32 v24, v23, v136, s[82:83]\n"
    "v_cndmask_b32 v25, v128, v138, s[82:83]\n"
    "v_cndmask_b32 v2, v24, v25, vcc\n"
    "v_cndmask_b32 v24, v22, v6, s[82:83]\n"
    "v_cndmask_b32 v25, v30, v8, s[82:83]\n"
    "v_cndmask_b32 v4, v24, v25, vcc\n"
    "v_cndmask_b32 v24, v23, v137, s[82:83]\n"
    "v_cndmask_b32 v25, v129, v139, s[82:83]\n"
    "v_cndmask_b32 v3, v24, v25, vcc\n"
    "v_cndmask_b32 v24, v23, v7, s[82:83]\n"
    "v_cndmask_b32 v25, v31, v9, s[82:83]\n"
    "v_cndmask_b32 v5, v24, v25, vcc\n"
    "s_add_u32 m0, m0, 0x4000\n"
    "s_nop 0\n"
    "buffer_load_dwordx4 v10, s[12:15], s86 offen offset:0 lds\n"
    "v_mul_f32 v144, v28, v26\n"
    "v_mul_f32 v145, v28, v27\n"
    "buffer_load_dwordx4 v11, s[12:15], s86 offen offset:1024 lds\n"
    "v_fma_f32 v144, -v29, v27, v144\n"
    "v_fmac_f32 v145, v29, v26\n"
    "v_mul_f32 v146, v144, v4\n"
    "v_mul_f32 v147, v144, v5\n"
    "v_fma_f32 v146, -v145, v5, v146\n"
    "v_fmac_f32 v147, v145, v4\n"
    "v_mul_f32 v148, v144, v2\n"
    "v_mul_f32 v149, v144, v3\n"
    "v_fma_f32 v148, -v145, v3, v148\n"
    "buffer_load_dwordx4 v12, s[12:15], s86 offen offset:2048 lds\n"
    "v_fmac_f32 v149, v145, v2\n"
    "v_xor_b32 v56, 64, v59\n"
    "v_and_b32 v22, 31, v1\n"
    "v_lshrrev_b32 v23, 5, v1\n"
    "v_and_b32 v24, 15, v1\n"
    "v_xor_b32 v23, v23, v24\n"
    "v_lshlrev_b32 v23, 4, v23\n"
    "v_lshl_or_b32 v22, v22, 8, v23\n"
    "s_lshl_b32 s4, s65, 13\n"
    "v_add_u32 v57, s4, v22\n"
    "buffer_load_dwordx4 v13, s[12:15], s86 offen offset:3072 lds\n"
    "v_xor_b32 v153, 32, v57\n"
    "v_xor_b32 v154, 64, v57\n"
    "v_xor_b32 v155, 96, v57\n"
    "v_xor_b32 v156, 128, v57\n"
    "v_xor_b32 v157, 160, v57\n"
    "v_xor_b32 v158, 192, v57\n"
    "v_xor_b32 v159, 224, v57\n"
    "v_mov_b32 v152, v57\n"
    "s_xor_b32 s84, s87, 3\n"
    "s_lshr_b32 s85, s84, 1\n"
    "s_lshl_b32 s85, s85, 8\n"
    "s_bitcmp1_b32 s84, 0\n"
    "s_cselect_b32 s12, s60, s58\n"
    "s_cselect_b32 s13, s61, s59\n"
    "s_add_u32 s12, s12, s85\n"
    "s_addc_u32 s13, s13, 0\n"
    "s_and_b32 s13, s13, 0xffff\n"
    "s_sub_u32 s14, 524288, s85\n"
    "s_add_u32 m0, s77, 98304\n"
    "s_mov_b32 s86, 65536\n"
    "buffer_load_dwordx4 v10, s[12:15], 0 offen lds\n"
    "v_fmac_f32 v65, v26, v64\n"
    "v_fmac_f32 v97, v26, v96\n"
    "v_fma_f32 v65, -v27, v96, v65\n"
    "v_fmac_f32 v97, v27, v64\n"
    "v_fmac_f32 v66, v26, v65\n"
    "v_fmac_f32 v98, v26, v97\n"
    "v_fma_f32 v66, -v27, v97, v66\n"
    "v_fmac_f32 v98, v27, v65\n"
    "v_fmac_f32 v67, v26, v66\n"
    "buffer_load_dwordx4 v11, s[12:15], 0 offen offset:1024 lds\n"
    "v_fmac_f32 v99, v26, v98\n"
    "v_fma_f32 v67, -v27, v98, v67\n"
    "v_fmac_f32 v99, v27, v66\n"
    "v_fmac_f32 v68, v26, v67\n"
    "v_fmac_f32 v100, v26, v99\n"
    "v_fma_f32 v68, -v27, v99, v68\n"
    "v_fmac_f32 v100, v27, v67\n"
    "v_fmac_f32 v69, v26, v68\n"
    "v_fmac_f32 v101, v26, v100\n"
    "v_fma_f32 v69, -v27, v100, v69\n"
    "buffer_load_dwordx4 v12, s[12:15], 0 offen offset:2048 lds\n"
    "v_fmac_f32 v101, v27, v68\n"
    "v_fmac_f32 v70, v26, v69\n"
    "v_fmac_f32 v102, v26, v101\n"
    "v_fma_f32 v70, -v27, v101, v70\n"
    "v_fmac_f32 v102, v27, v69\n"
    "v_fmac_f32 v71, v26, v70\n"
    "v_fmac_f32 v103, v26, v102\n"
    "v_fma_f32 v71, -v27, v102, v71\n"
    "v_fmac_f32 v103, v27, v70\n"
    "buffer_load_dwordx4 v13, s[12:15], 0 offen offset:3072 lds\n"
    "v_fmac_f32 v72, v26, v71\n"
    "v_fmac_f32 v104, v26, v103\n"
    "v_fma_f32 v72, -v27, v103, v72\n"
    "v_fmac_f32 v104, v27, v71\n"
    "v_fmac_f32 v73, v26, v72\n"
    "v_fmac_f32 v105, v26, v104\n"
    "v_fma_f32 v73, -v27, v104, v73\n"
    "v_fmac_f32 v105, v27, v72\n"
    "v_fmac_f32 v74, v26, v73\n"
    "s_add_u32 m0, m0, 0x4000\n"
    "s_nop 0\n"
    "buffer_load_dwordx4 v10, s[12:15], s86 offen offset:0 lds\n"
    "v_fmac_f32 v106, v26, v105\n"
    "v_fma_f32 v74, -v27, v105, v74\n"
    "v_fmac_f32 v106, v27, v73\n"
    "v_fmac_f32 v75, v26, v74\n"
    "v_fmac_f32 v107, v26, v106\n"
    "v_fma_f32 v75, -v27, v106, v75\n"
    "v_fmac_f32 v107, v27, v74\n"
    "v_fmac_f32 v76, v26, v75\n"
    "v_fmac_f32 v108, v26, v107\n"
    "buffer_load_dwordx4 v11, s[12:15], s86 offen offset:1024 lds\n"
    "v_fma_f32 v76, -v27, v107, v76\n"
    "v_fmac_f32 v108, v27, v75\n"
    "v_fmac_f32 v77, v26, v76\n"
    "v_fmac_f32 v109, v26, v108\n"
    "v_fma_f32 v77, -v27, v108, v77\n"
    "v_fmac_f32 v109, v27, v76\n"
    "v_fmac_f32 v78, v26, v77\n"
    "v_fmac_f32 v110, v26, v109\n"
    "v_fma_f32 v78, -v27, v109, v78\n"
    "buffer_load_dwordx4 v12, s[12:15], s86 offen offset:2048 lds\n"
    "v_fmac_f32 v110, v27, v77\n"
    "v_fmac_f32 v79, v26, v78\n"
    "v_fmac_f32 v111, v26, v110\n"
    "v_fma_f32 v79, -v27, v110, v79\n"
    "v_fmac_f32 v111, v27, v78\n"
    "v_fmac_f32 v80, v26, v79\n"
    "v_fmac_f32 v112, v26, v111\n"
    "v_fma_f32 v80, -v27, v111, v80\n"
    "v_fmac_f32 v112, v27, v79\n"
    "v_fmac_f32 v81, v26, v80\n"
    "buffer_load_dwordx4 v13, s[12:15], s86 offen offset:3072 lds\n"
    "v_fmac_f32 v113, v26, v112\n"
    "v_fma_f32 v81, -v27, v112, v81\n"
    "v_fmac_f32 v113, v27, v80\n"
    "v_fmac_f32 v82, v26, v81\n"
    "v_fmac_f32 v114, v26, v113\n"
    "v_fma_f32 v82, -v27, v113, v82\n"
    "v_fmac_f32 v114, v27, v81\n"
    "v_fmac_f32 v83, v26, v82\n"
    "v_fmac_f32 v115, v26, v114\n"
    "s_add_u32 m0, s77, 147456\n"
    "s_nop 0\n"
    "buffer_load_dwordx4 v18, s[8:11], 0 offen offset:0 lds\n"
    "v_fma_f32 v83, -v27, v114, v83\n"
    "v_fmac_f32 v115, v27, v82\n"
    "v_fmac_f32 v84, v26, v83\n"
    "v_fmac_f32 v116, v26, v115\n"
    "v_fma_f32 v84, -v27, v115, v84\n"
    "v_fmac_f32 v116, v27, v83\n"
    "v_fmac_f32 v85, v26, v84\n"
    "v_fmac_f32 v117, v26, v116\n"
    "v_fma_f32 v85, -v27, v116, v85\n"
    "buffer_load_dwordx4 v19, s[8:11], 0 offen offset:1024 lds\n"
    "v_fmac_f32 v117, v27, v84\n"
    "v_fmac_f32 v86, v26, v85\n"
    "v_fmac_f32 v118, v26, v117\n"
    "v_fma_f32 v86, -v27, v117, v86\n"
    "v_fmac_f32 v118, v27, v85\n"
    "v_fmac_f32 v87, v26, v86\n"
    "v_fmac_f32 v119, v26, v118\n"
    "v_fma_f32 v87, -v27, v118, v87\n"
    "v_fmac_f32 v119, v27, v86\n"
    "buffer_load_dwordx4 v20, s[8:11], 0 offen offset:2048 lds\n"
    "v_fmac_f32 v88, v26, v87\n"
    "v_fmac_f32 v120, v26, v119\n"
    "v_fma_f32 v88, -v27, v119, v88\n"
    "v_fmac_f32 v120, v27, v87\n"
    "v_fmac_f32 v89, v26, v88\n"
    "v_fmac_f32 v121, v26, v120\n"
    "v_fma_f32 v89, -v27, v120, v89\n"
    "v_fmac_f32 v121, v27, v88\n"
    "v_fmac_f32 v90, v26, v89\n"
    "buffer_load_dwordx4 v21, s[8:11], 0 offen offset:3072 lds\n"
    "v_fmac_f32 v122, v26, v121\n"
    "v_fma_f32 v90, -v27, v121, v90\n"
    "v_fmac_f32 v122, v27, v89\n"
    "v_fmac_f32 v91, v26, v90\n"
    "v_fmac_f32 v123, v26, v122\n"
    "v_fma_f32 v91, -v27, v122, v91\n"
    "v_fmac_f32 v123, v27, v90\n"
    "v_fmac_f32 v92, v26, v91\n"
    "v_fmac_f32 v124, v26, v123\n"
    "v_fma_f32 v92, -v27, v123, v92\n"
    "s_waitcnt vmcnt(20)\n"
    "s_barrier\n"
    "v_fmac_f32 v124, v27, v91\n"
    "ds_read_b128 a[0:3], v152 offset:0\n"
    "v_fmac_f32 v93, v26, v92\n"
    "v_fmac_f32 v125, v26, v124\n"
    "ds_read_b128 a[4:7], v153 offset:0\n"
    "v_fma_f32 v93, -v27, v124, v93\n"
    "v_fmac_f32 v125, v27, v92\n"
    "ds_read_b128 a[8:11], v154 offset:0\n"
    "v_fmac_f32 v94, v26, v93\n"
    "v_fmac_f32 v126, v26, v125\n"
    "ds_read_b128 a[12:15], v155 offset:0\n"
    "v_fma_f32 v94, -v27, v125, v94\n"
    "v_fmac_f32 v126, v27, v93\n"
    "ds_read_b128 a[16:19], v156 offset:0\n"
    "v_fmac_f32 v95, v26, v94\n"
    "v_fmac_f32 v127, v26, v126\n"
    "ds_read_b128 a[20:23], v157 offset:0\n"
    "v_fma_f32 v95, -v27, v126, v95\n"
    "v_fmac_f32 v127, v27, v94\n"
    "v_pk_mul_f32 v[136:137], v[64:65], v[28:29] op_sel_hi:[1,0]\n"
    "ds_read_b128 a[24:27], v158 offset:0\n"
    "v_pk_fma_f32 v[136:137], v[96:97], v[28:29], v[136:137] op_sel:[0,1,0] op_sel_hi:[1,1,1] neg_lo:[1,0,0] neg_hi:[1,0,0]\n"
    "v_pk_mul_f32 v[96:97], v[96:97], v[28:29] op_sel_hi:[1,0] neg_lo:[1,0] neg_hi:[1,0]\n"
    "ds_read_b128 a[28:31], v159 offset:0\n"
    "v_pk_fma_f32 v[96:97], v[64:65], v[28:29], v[96:97] op_sel:[0,1,0] op_sel_hi:[1,1,1] neg_lo:[1,0,0] neg_hi:[1,0,0]\n"
    "v_pk_mul_f32 v[64:65], v[66:67], v[28:29] op_sel_hi:[1,0]\n"
    "ds_read_b128 a[32:35], v152 offset:32768\n"
    "v_pk_fma_f32 v[64:65], v[98:99], v[28:29], v[64:65] op_sel:[0,1,0] op_sel_hi:[1,1,1] neg_lo:[1,0,0] neg_hi:[1,0,0]\n"
    "v_pk_mul_f32 v[98:99], v[98:99], v[28:29] op_sel_hi:[1,0] neg_lo:[1,0] neg_hi:[1,0]\n"
    "ds_read_b128 a[36:39], v153 offset:32768\n"
    "v_pk_fma_f32 v[98:99], v[66:67], v[28:29], v[98:99] op_sel:[0,1,0] op_sel_hi:[1,1,1] neg_lo:[1,0,0] neg_hi:[1,0,0]\n"
    "v_pk_mul_f32 v[66:67], v[68:69], v[28:29] op_sel_hi:[1,0]\n"
    "ds_read_b128 a[40:43], v154 offset:32768\n"
    "v_pk_fma_f32 v[66:67], v[100:101], v[28:29], v[66:67] op_sel:[0,1,0] op_sel_hi:[1,1,1] neg_lo:[1,0,0] neg_hi:[1,0,0]\n"
    "v_pk_mul_f32 v[100:101], v[100:101], v[28:29] op_sel_hi:[1,0] neg_lo:[1,0] neg_hi:[1,0]\n"
    "ds_read_b128 a[44:47], v155 offset:32768\n"
    "v_pk_fma_f32 v[100:101], v[68:69], v[28:29], v[100:101] op_sel:[0,1,0] op_sel_hi:[1,1,1] neg_lo:[1,0,0] neg_hi:[1,0,0]\n"
    "v_pk_mul_f32 v[68:69], v[70:71], v[28:29] op_sel_hi:[1,0]\n"
    "v_pk_fma_f32 v[68:69], v[102:103], v[28:29], v[68:69] op_sel:[0,1,0] op_sel_hi:[1,1,1] neg_lo:[1,0,0] neg_hi:[1,0,0]\n"
    "ds_read_b128 a[48:51], v156 offset:32768\n"
    "v_pk_mul_f32 v[102:103], v[102:103], v[28:29] op_sel_hi:[1,0] neg_lo:[1,0] neg_hi:[1,0]\n"
    "v_pk_fma_f32 v[102:103], v[70:71], v[28:29], v[102:103] op_sel:[0,1,0] op_sel_hi:[1,1,1] neg_lo:[1,0,0] neg_hi:[1,0,0]\n"
    "ds_read_b128 a[52:55], v157 offset:32768\n"
    "v_pk_mul_f32 v[70:71], v[72:73], v[28:29] op_sel_hi:[1,0]\n"
    "v_pk_fma_f32 v[70:71], v[104:105], v[28:29], v[70:71] op_sel:[0,1,0] op_sel_hi:[1,1,1] neg_lo:[1,0,0] neg_hi:[1,0,0]\n"
    "ds_read_b128 a[56:59], v158 offset:32768\n"
    "v_pk_mul_f32 v[104:105], v[104:105], v[28:29] op_sel_hi:[1,0] neg_lo:[1,0] neg_hi:[1,0]\n"
    "v_pk_fma_f32 v[104:105], v[72:73], v[28:29], v[104:105] op_sel:[0,1,0] op_sel_hi:[1,1,1] neg_lo:[1,0,0] neg_hi:[1,0,0]\n"
    "ds_read_b128 a[60:63], v159 offset:32768\n"
    "v_pk_mul_f32 v[72:73], v[74:75], v[28:29] op_sel_hi:[1,0]\n"
    "v_pk_fma_f32 v[72:73], v[106:107], v[28:29], v[72:73] op_sel:[0,1,0] op_sel_hi:[1,1,1] neg_lo:[1,0,0] neg_hi:[1,0,0]\n"
    "v_xor_b32 v152, 0x10000, v152\n"
    "v_pk_mul_f32 v[106:107], v[106:107], v[28:29] op_sel_hi:[1,0] neg_lo:[1,0] neg_hi:[1,0]\n"
    "v_pk_fma_f32 v[106:107], v[74:75], v[28:29], v[106:107] op_sel:[0,1,0] op_sel_hi:[1,1,1] neg_lo:[1,0,0] neg_hi:[1,0,0]\n"
    "v_xor_b32 v153, 0x10000, v153\n"
    "v_pk_mul_f32 v[74:75], v[76:77], v[28:29] op_sel_hi:[1,0]\n"
    "v_pk_fma_f32 v[74:75], v[108:109], v[28:29], v[74:75] op_sel:[0,1,0] op_sel_hi:[1,1,1] neg_lo:[1,0,0] neg_hi:[1,0,0]\n"
    "v_pk_mul_f32 v[108:109], v[108:109], v[28:29] op_sel_hi:[1,0] neg_lo:[1,0] neg_hi:[1,0]\n"
    "v_xor_b32 v154, 0x10000, v154\n"
    "v_pk_fma_f32 v[108:109], v[76:77], v[28:29], v[108:109] op_sel:[0,1,0] op_sel_hi:[1,1,1] neg_lo:[1,0,0] neg_hi:[1,0,0]\n"
    "v_pk_mul_f32 v[76:77], v[78:79], v[28:29] op_sel_hi:[1,0]\n"
    "v_xor_b32 v155, 0x10000, v155\n"
    "v_pk_fma_f32 v[76:77], v[110:111], v[28:29], v[76:77] op_sel:[0,1,0] op_sel_hi:[1,1,1] neg_lo:[1,0,0] neg_hi:[1,0,0]\n"
    "v_pk_mul_f32 v[110:111], v[110:111], v[28:29] op_sel_hi:[1,0] neg_lo:[1,0] neg_hi:[1,0]\n"
    "v_xor_b32 v156, 0x10000, v156\n"
    "v_pk_fma_f32 v[110:111], v[78:79], v[28:29], v[110:111] op_sel:[0,1,0] op_sel_hi:[1,1,1] neg_lo:[1,0,0] neg_hi:[1,0,0]\n"
    "v_pk_mul_f32 v[78:79], v[80:81], v[28:29] op_sel_hi:[1,0]\n"
    "v_xor_b32 v157, 0x10000, v157\n"
    "v_pk_fma_f32 v[78:79], v[112:113], v[28:29], v[78:79] op_sel:[0,1,0] op_sel_hi:[1,1,1] neg_lo:[1,0,0] neg_hi:[1,0,0]\n"
    "v_pk_mul_f32 v[112:113], v[112:113], v[28:29] op_sel_hi:[1,0] neg_lo:[1,0] neg_hi:[1,0]\n"
    "v_xor_b32 v158, 0x10000, v158\n"
    "v_pk_fma_f32 v[112:113], v[80:81], v[28:29], v[112:113] op_sel:[0,1,0] op_sel_hi:[1,1,1] neg_lo:[1,0,0] neg_hi:[1,0,0]\n"
    "v_pk_mul_f32 v[80:81], v[82:83], v[28:29] op_sel_hi:[1,0]\n"
    "v_xor_b32 v159, 0x10000, v159\n"
    "v_pk_fma_f32 v[80:81], v[114:115], v[28:29], v[80:81] op_sel:[0,1,0] op_sel_hi:[1,1,1] neg_lo:[1,0,0] neg_hi:[1,0,0]\n"
    "v_pk_mul_f32 v[114:115], v[114:115], v[28:29] op_sel_hi:[1,0] neg_lo:[1,0] neg_hi:[1,0]\n"
    "v_pk_fma_f32 v[114:115], v[82:83], v[28:29], v[114:115] op_sel:[0,1,0] op_sel_hi:[1,1,1] neg_lo:[1,0,0] neg_hi:[1,0,0]\n"
    "buffer_load_dwordx4 v[160:163], v48, s[28:31], 0 offen sc1\n"
    "buffer_load_dwordx4 v[164:167], v49, s[28:31], 0 offen sc1\n"
    "buffer_load_dwordx4 v[168:171], v50, s[28:31], 0 offen sc1\n"
    "buffer_load_dwordx4 v[172:175], v51, s[28:31], 0 offen sc1\n"
    "buffer_load_dwordx4 v[176:179], v52, s[28:31], 0 offen sc1\n"
    "buffer_load_dwordx4 v[180:183], v53, s[28:31], 0 offen sc1\n"
    "buffer_load_dwordx4 v[184:187], v54, s[28:31], 0 offen sc1\n"
    "buffer_load_dwordx4 v[188:191], v55, s[28:31], 0 offen sc1\n"
    "v_pk_mul_f32 v[82:83], v[84:85], v[28:29] op_sel_hi:[1,0]\n"
    "v_pk_fma_f32 v[82:83], v[116:117], v[28:29], v[82:83] op_sel:[0,1,0] op_sel_hi:[1,1,1] neg_lo:[1,0,0] neg_hi:[1,0,0]\n"
    "v_pk_mul_f32 v[116:117], v[116:117], v[28:29] op_sel_hi:[1,0] neg_lo:[1,0] neg_hi:[1,0]\n"
    "v_pk_fma_f32 v[116:117], v[84:85], v[28:29], v[116:117] op_sel:[0,1,0] op_sel_hi:[1,1,1] neg_lo:[1,0,0] neg_hi:[1,0,0]\n"
    "v_pk_mul_f32 v[84:85], v[86:87], v[28:29] op_sel_hi:[1,0]\n"
    "v_pk_fma_f32 v[84:85], v[118:119], v[28:29], v[84:85] op_sel:[0,1,0] op_sel_hi:[1,1,1] neg_lo:[1,0,0] neg_hi:[1,0,0]\n"
    "v_pk_mul_f32 v[118:119], v[118:119], v[28:29] op_sel_hi:[1,0] neg_lo:[1,0] neg_hi:[1,0]\n"
    "v_pk_fma_f32 v[118:119], v[86:87], v[28:29], v[118:119] op_sel:[0,1,0] op_sel_hi:[1,1,1] neg_lo:[1,0,0] neg_hi:[1,0,0]\n"
    "v_pk_mul_f32 v[86:87], v[88:89], v[28:29] op_sel_hi:[1,0]\n"
    "v_pk_fma_f32 v[86:87], v[120:121], v[28:29], v[86:87] op_sel:[0,1,0] op_sel_hi:[1,1,1] neg_lo:[1,0,0] neg_hi:[1,0,0]\n"
    "v_pk_mul_f32 v[120:121], v[120:121], v[28:29] op_sel_hi:[1,0] neg_lo:[1,0] neg_hi:[1,0]\n"
    "v_pk_fma_f32 v[120:121], v[88:89], v[28:29], v[120:121] op_sel:[0,1,0] op_sel_hi:[1,1,1] neg_lo:[1,0,0] neg_hi:[1,0,0]\n"
    "v_pk_mul_f32 v[88:89], v[90:91], v[28:29] op_sel_hi:[1,0]\n"
    "v_pk_fma_f32 v[88:89], v[122:123], v[28:29], v[88:89] op_sel:[0,1,0] op_sel_hi:[1,1,1] neg_lo:[1,0,0] neg_hi:[1,0,0]\n"
    "v_pk_mul_f32 v[122:123], v[122:123], v[28:29] op_sel_hi:[1,0] neg_lo:[1,0] neg_hi:[1,0]\n"
    "v_pk_fma_f32 v[122:123], v[90:91], v[28:29], v[122:123] op_sel:[0,1,0] op_sel_hi:[1,1,1] neg_lo:[1,0,0] neg_hi:[1,0,0]\n"
    "v_pk_mul_f32 v[90:91], v[92:93], v[28:29] op_sel_hi:[1,0]\n"
    "v_pk_fma_f32 v[90:91], v[124:125], v[28:29], v[90:91] op_sel:[0,1,0] op_sel_hi:[1,1,1] neg_lo:[1,0,0] neg_hi:[1,0,0]\n"
    "v_pk_mul_f32 v[124:125], v[124:125], v[28:29] op_sel_hi:[1,0] neg_lo:[1,0] neg_hi:[1,0]\n"
    "v_pk_fma_f32 v[124:125], v[92:93], v[28:29], v[124:125] op_sel:[0,1,0] op_sel_hi:[1,1,1] neg_lo:[1,0,0] neg_hi:[1,0,0]\n"
    "v_pk_mul_f32 v[92:93], v[94:95], v[28:29] op_sel_hi:[1,0]\n"
    "v_pk_fma_f32 v[92:93], v[126:127], v[28:29], v[92:93] op_sel:[0,1,0] op_sel_hi:[1,1,1] neg_lo:[1,0,0] neg_hi:[1,0,0]\n"
    "v_pk_mul_f32 v[126:127], v[126:127], v[28:29] op_sel_hi:[1,0] neg_lo:[1,0] neg_hi:[1,0]\n"
    "v_pk_fma_f32 v[126:127], v[94:95], v[28:29], v[126:127] op_sel:[0,1,0] op_sel_hi:[1,1,1] neg_lo:[1,0,0] neg_hi:[1,0,0]\n"
    "s_waitcnt lgkmcnt(0)\n"
    "s_barrier\n"
    "s_xor_b32 s84, s87, 4\n"
    "s_lshr_b32 s85, s84, 1\n"
    "s_lshl_b32 s85, s85, 8\n"
    "s_bitcmp1_b32 s84, 0\n"
    "s_cselect_b32 s12, s60, s58\n"
    "s_cselect_b32 s13, s61, s59\n"
    "s_add_u32 s12, s12, s85\n"
    "s_addc_u32 s13, s13, 0\n"
    "s_and_b32 s13, s13, 0xffff\n"
    "s_sub_u32 s14, 524288, s85\n"
    "s_add_u32 m0, s77, 0\n"
    "s_mov_b32 s86, 65536\n"
    "buffer_load_dwordx4 v10, s[12:15], 0 offen lds\n"
    "buffer_load_dwordx4 v11, s[12:15], 0 offen offset:1024 lds\n"
    "buffer_load_dwordx4 v12, s[12:15], 0 offen offset:2048 lds\n"
    "buffer_load_dwordx4 v13, s[12:15], 0 offen offset:3072 lds\n"
    "s_add_u32 m0, m0, 0x4000\n"
    "s_nop 0\n"
    "buffer_load_dwordx4 v10, s[12:15], s86 offen offset:0 lds\n"
    "buffer_load_dwordx4 v11, s[12:15], s86 offen offset:1024 lds\n"
    "buffer_load_dwordx4 v12, s[12:15], s86 offen offset:2048 lds\n"
    "buffer_load_dwordx4 v13, s[12:15], s86 offen offset:3072 lds\n"
    "s_mov_b32 s79, 65536\n"
    "s_waitcnt vmcnt(8)\n"
    "s_branch Lg1_5%=\n"
    "Lg_3%=:\n"
    "s_waitcnt vmcnt(0)\n"
    "Lg1_5%=:\n"
    "v_cmpx_eq_u32 vcc, s80, v161\n"
    "v_cmpx_eq_u32 vcc, s81, v163\n"
    "v_cmpx_eq_u32 vcc, s80, v165\n"
    "v_cmpx_eq_u32 vcc, s81, v167\n"
    "v_cmpx_eq_u32 vcc, s80, v169\n"
    "v_cmpx_eq_u32 vcc, s81, v171\n"
    "v_cmpx_eq_u32 vcc, s80, v173\n"
    "v_cmpx_eq_u32 vcc, s81, v175\n"
    "v_cmpx_eq_u32 vcc, s80, v177\n"
    "v_cmpx_eq_u32 vcc, s81, v179\n"
    "v_cmpx_eq_u32 vcc, s80, v181\n"
    "v_cmpx_eq_u32 vcc, s81, v183\n"
    "v_cmpx_eq_u32 vcc, s80, v185\n"
    "v_cmpx_eq_u32 vcc, s81, v187\n"
    "v_cmpx_eq_u32 vcc, s80, v189\n"
    "v_cmpx_eq_u32 vcc, s81, v191\n"
    "s_nop 0\n"
    "s_cmp_eq_u64 exec, -1\n"
    "s_mov_b64 exec, -1\n"
    "s_cbranch_scc1 Lgd_4%=\n"
    "s_sub_u32 s79, s79, 1\n"
    "s_cbranch_scc1 Lgd_4%=\n"
    "buffer_load_dwordx4 v[160:163], v48, s[28:31], 0 offen sc1\n"
    "buffer_load_dwordx4 v[164:167], v49, s[28:31], 0 offen sc1\n"
    "buffer_load_dwordx4 v[168:171], v50, s[28:31], 0 offen sc1\n"
    "buffer_load_dwordx4 v[172:175], v51, s[28:31], 0 offen sc1\n"
    "buffer_load_dwordx4 v[176:179], v52, s[28:31], 0 offen sc1\n"
    "buffer_load_dwordx4 v[180:183], v53, s[28:31], 0 offen sc1\n"
    "buffer_load_dwordx4 v[184:187], v54, s[28:31], 0 offen sc1\n"
    "buffer_load_dwordx4 v[188:191], v55, s[28:31], 0 offen sc1\n"
    "s_branch Lg_3%=\n"
    "Lgd_4%=:\n"
    "v_pk_mul_f32 v[2:3], v[32:33], v[160:161] op_sel_hi:[1,0]\n"
    "v_pk_mul_f32 v[4:5], v[32:33], v[162:163] op_sel:[1,0] op_sel_hi:[0,0] neg_lo:[1,0]\n"
    "v_pk_fma_f32 v[2:3], v[34:35], v[164:165], v[2:3] op_sel_hi:[1,0,1]\n"
    "v_pk_fma_f32 v[4:5], v[34:35], v[166:167], v[4:5] op_sel:[1,0,0] op_sel_hi:[0,0,1] neg_lo:[1,0,0]\n"
    "v_pk_fma_f32 v[2:3], v[36:37], v[168:169], v[2:3] op_sel_hi:[1,0,1]\n"
    "v_pk_fma_f32 v[4:5], v[36:37], v[170:171], v[4:5] op_sel:[1,0,0] op_sel_hi:[0,0,1] neg_lo:[1,0,0]\n"
    "v_pk_fma_f32 v[2:3], v[38:39], v[172:173], v[2:3] op_sel_hi:[1,0,1]\n"
    "v_pk_fma_f32 v[4:5], v[38:39], v[174:175], v[4:5] op_sel:[1,0,0] op_sel_hi:[0,0,1] neg_lo:[1,0,0]\n"
    "v_pk_fma_f32 v[2:3], v[40:41], v[176:177], v[2:3] op_sel_hi:[1,0,1]\n"
    "v_pk_fma_f32 v[4:5], v[40:41], v[178:179], v[4:5] op_sel:[1,0,0] op_sel_hi:[0,0,1] neg_lo:[1,0,0]\n"
    "v_pk_fma_f32 v[2:3], v[42:43], v[180:181], v[2:3] op_sel_hi:[1,0,1]\n"
    "v_pk_fma_f32 v[4:5], v[42:43], v[182:183], v[4:5] op_sel:[1,0,0] op_sel_hi:[0,0,1] neg_lo:[1,0,0]\n"
    "v_pk_fma_f32 v[2:3], v[44:45], v[184:185], v[2:3] op_sel_hi:[1,0,1]\n"
    "v_pk_fma_f32 v[4:5], v[44:45], v[186:187], v[4:5] op_sel:[1,0,0] op_sel_hi:[0,0,1] neg_lo:[1,0,0]\n"
    "v_pk_fma_f32 v[2:3], v[46:47], v[188:189], v[2:3] op_sel_hi:[1,0,1]\n"
    "v_pk_fma_f32 v[4:5], v[46:47], v[190:191], v[4:5] op_sel:[1,0,0] op_sel_hi:[0,0,1] neg_lo:[1,0,0]\n"
    "v_pk_add_f32 v[2:3], v[2:3], v[4:5]\n"
    "s_mov_b32 s4, 0\n"
    "v_mov_b64 v[4:5], v[2:3]\n"
    "s_mov_b32 s5, 0x20000\n"
    "s_nop 0\n"
    "v_permlane32_swap_b32 v2, v4\n"
    "v_permlane32_swap_b32 v3, v5\n"
    "v_pk_add_f32 v[2:3], v[2:3], v[4:5]\n"
    "v_fma_f32 v148, v146, v2, v148\n"
    "v_fma_f32 v149, v146, v3, v149\n"
    "v_fma_f32 v148, -v147, v3, v148\n"
    "v_fmac_f32 v149, v147, v2\n"
    "v_pk_fma_f32 v[136:137], v[192:193], v[148:149], v[136:137] op_sel_hi:[1,0,1]\n"
    "v_pk_fma_f32 v[96:97], v[192:193], v[148:149], v[96:97] op_sel:[0,1,0] op_sel_hi:[1,1,1] neg_lo:[1,0,0] neg_hi:[1,0,0]\n"
    "v_pk_fma_f32 v[64:65], v[194:195], v[148:149], v[64:65] op_sel_hi:[1,0,1]\n"
    "v_pk_fma_f32 v[98:99], v[194:195], v[148:149], v[98:99] op_sel:[0,1,0] op_sel_hi:[1,1,1] neg_lo:[1,0,0] neg_hi:[1,0,0]\n"
    "v_pk_fma_f32 v[66:67], v[196:197], v[148:149], v[66:67] op_sel_hi:[1,0,1]\n"
    "v_pk_fma_f32 v[100:101], v[196:197], v[148:149], v[100:101] op_sel:[0,1,0] op_sel_hi:[1,1,1] neg_lo:[1,0,0] neg_hi:[1,0,0]\n"
    "v_pk_fma_f32 v[68:69], v[198:199], v[148:149], v[68:69] op_sel_hi:[1,0,1]\n"
    "v_pk_fma_f32 v[102:103], v[198:199], v[148:149], v[102:103] op_sel:[0,1,0] op_sel_hi:[1,1,1] neg_lo:[1,0,0] neg_hi:[1,0,0]\n"
    "v_pk_fma_f32 v[136:137], v[224:225], v[148:149], v[136:137] op_sel:[0,1,0] op_sel_hi:[1,1,1] neg_lo:[1,0,0] neg_hi:[1,0,0]\n"
    "v_pk_fma_f32 v[96:97], v[224:225], v[148:149], v[96:97] op_sel_hi:[1,0,1] neg_lo:[1,0,0] neg_hi:[1,0,0]\n"
    "v_pk_fma_f32 v[64:65], v[226:227], v[148:149], v[64:65] op_sel:[0,1,0] op_sel_hi:[1,1,1] neg_lo:[1,0,0] neg_hi:[1,0,0]\n"
    "v_pk_fma_f32 v[98:99], v[226:227], v[148:149], v[98:99] op_sel_hi:[1,0,1] neg_lo:[1,0,0] neg_hi:[1,0,0]\n"
    "v_pk_fma_f32 v[66:67], v[228:229], v[148:149], v[66:67] op_sel:[0,1,0] op_sel_hi:[1,1,1] neg_lo:[1,0,0] neg_hi:[1,0,0]\n"
    "v_pk_fma_f32 v[100:101], v[228:229], v[148:149], v[100:101] op_sel_hi:[1,0,1] neg_lo:[1,0,0] neg_hi:[1,0,0]\n"
    "v_pk_fma_f32 v[68:69], v[230:231], v[148:149], v[68:69] op_sel:[0,1,0] op_sel_hi:[1,1,1] neg_lo:[1,0,0] neg_hi:[1,0,0]\n"
    "v_pk_fma_f32 v[102:103], v[230:231], v[148:149], v[102:103] op_sel_hi:[1,0,1] neg_lo:[1,0,0] neg_hi:[1,0,0]\n"
    "v_cvt_pk_bf16_f32 v160, v136, v137\n"
    "v_cvt_pk_bf16_f32 v161, v64, v65\n"
    "v_cvt_pk_bf16_f32 v162, v66, v67\n"
    "v_cvt_pk_bf16_f32 v163, v68, v69\n"
    "buffer_store_dwordx4 v[160:163], v59, s[20:23], s4 offen offset:0 sc1\n"
    "v_cvt_pk_bf16_f32 v164, v96, v97\n"
    "v_cvt_pk_bf16_f32 v165, v98, v99\n"
    "v_cvt_pk_bf16_f32 v166, v100, v101\n"
    "v_cvt_pk_bf16_f32 v167, v102, v103\n"
    "buffer_store_dwordx4 v[164:167], v59, s[20:23], s4 offen offset:2048 sc1\n"
    "v_pk_fma_f32 v[70:71], v[200:201], v[148:149], v[70:71] op_sel_hi:[1,0,1]\n"
    "v_pk_fma_f32 v[104:105], v[200:201], v[148:149], v[104:105] op_sel:[0,1,0] op_sel_hi:[1,1,1] neg_lo:[1,0,0] neg_hi:[1,0,0]\n"
    "v_pk_fma_f32 v[72:73], v[202:203], v[148:149], v[72:73] op_sel_hi:[1,0,1]\n"
    "v_pk_fma_f32 v[106:107], v[202:203], v[148:149], v[106:107] op_sel:[0,1,0] op_sel_hi:[1,1,1] neg_lo:[1,0,0] neg_hi:[1,0,0]\n"
    "v_pk_fma_f32 v[74:75], v[204:205], v[148:149], v[74:75] op_sel_hi:[1,0,1]\n"
    "v_pk_fma_f32 v[108:109], v[204:205], v[148:149], v[108:109] op_sel:[0,1,0] op_sel_hi:[1,1,1] neg_lo:[1,0,0] neg_hi:[1,0,0]\n"
    "v_pk_fma_f32 v[76:77], v[206:207], v[148:149], v[76:77] op_sel_hi:[1,0,1]\n"
    "v_pk_fma_f32 v[110:111], v[206:207], v[148:149], v[110:111] op_sel:[0,1,0] op_sel_hi:[1,1,1] neg_lo:[1,0,0] neg_hi:[1,0,0]\n"
    "v_pk_fma_f32 v[70:71], v[232:233], v[148:149], v[70:71] op_sel:[0,1,0] op_sel_hi:[1,1,1] neg_lo:[1,0,0] neg_hi:[1,0,0]\n"
    "v_pk_fma_f32 v[104:105], v[232:233], v[148:149], v[104:105] op_sel_hi:[1,0,1] neg_lo:[1,0,0] neg_hi:[1,0,0]\n"
    "v_pk_fma_f32 v[72:73], v[234:235], v[148:149], v[72:73] op_sel:[0,1,0] op_sel_hi:[1,1,1] neg_lo:[1,0,0] neg_hi:[1,0,0]\n"
    "v_pk_fma_f32 v[106:107], v[234:235], v[148:149], v[106:107] op_sel_hi:[1,0,1] neg_lo:[1,0,0] neg_hi:[1,0,0]\n"
    "v_pk_fma_f32 v[74:75], v[236:237], v[148:149], v[74:75] op_sel:[0,1,0] op_sel_hi:[1,1,1] neg_lo:[1,0,0] neg_hi:[1,0,0]\n"
    "v_pk_fma_f32 v[108:109], v[236:237], v[148:149], v[108:109] op_sel_hi:[1,0,1] neg_lo:[1,0,0] neg_hi:[1,0,0]\n"
    "v_pk_fma_f32 v[76:77], v[238:239], v[148:149], v[76:77] op_sel:[0,1,0] op_sel_hi:[1,1,1] neg_lo:[1,0,0] neg_hi:[1,0,0]\n"
    "v_pk_fma_f32 v[110:111], v[238:239], v[148:149], v[110:111] op_sel_hi:[1,0,1] neg_lo:[1,0,0] neg_hi:[1,0,0]\n"
    "v_cvt_pk_bf16_f32 v168, v70, v71\n"
    "v_cvt_pk_bf16_f32 v169, v72, v73\n"
    "v_cvt_pk_bf16_f32 v170, v74, v75\n"
    "v_cvt_pk_bf16_f32 v171, v76, v77\n"
    "buffer_store_dwordx4 v[168:171], v56, s[20:23], s4 offen offset:1024 sc1\n"
    "v_cvt_pk_bf16_f32 v172, v104, v105\n"
    "v_cvt_pk_bf16_f32 v173, v106, v107\n"
    "v_cvt_pk_bf16_f32 v174, v108, v109\n"
    "v_cvt_pk_bf16_f32 v175, v110, v111\n"
    "buffer_store_dwordx4 v[172:175], v56, s[20:23], s4 offen offset:3072 sc1\n"
    "v_pk_fma_f32 v[78:79], v[208:209], v[148:149], v[78:79] op_sel_hi:[1,0,1]\n"
    "v_pk_fma_f32 v[112:113], v[208:209], v[148:149], v[112:113] op_sel:[0,1,0] op_sel_hi:[1,1,1] neg_lo:[1,0,0] neg_hi:[1,0,0]\n"
    "v_pk_fma_f32 v[80:81], v[210:211], v[148:149], v[80:81] op_sel_hi:[1,0,1]\n"
    "v_pk_fma_f32 v[114:115], v[210:211], v[148:149], v[114:115] op_sel:[0,1,0] op_sel_hi:[1,1,1] neg_lo:[1,0,0] neg_hi:[1,0,0]\n"
    "v_pk_fma_f32 v[82:83], v[212:213], v[148:149], v[82:83] op_sel_hi:[1,0,1]\n"
    "v_pk_fma_f32 v[116:117], v[212:213], v[148:149], v[116:117] op_sel:[0,1,0] op_sel_hi:[1,1,1] neg_lo:[1,0,0] neg_hi:[1,0,0]\n"
    "v_pk_fma_f32 v[84:85], v[214:215], v[148:149], v[84:85] op_sel_hi:[1,0,1]\n"
    "v_pk_fma_f32 v[118:119], v[214:215], v[148:149], v[118:119] op_sel:[0,1,0] op_sel_hi:[1,1,1] neg_lo:[1,0,0] neg_hi:[1,0,0]\n"
    "v_pk_fma_f32 v[78:79], v[240:241], v[148:149], v[78:79] op_sel:[0,1,0] op_sel_hi:[1,1,1] neg_lo:[1,0,0] neg_hi:[1,0,0]\n"
    "v_pk_fma_f32 v[112:113], v[240:241], v[148:149], v[112:113] op_sel_hi:[1,0,1] neg_lo:[1,0,0] neg_hi:[1,0,0]\n"
    "v_pk_fma_f32 v[80:81], v[242:243], v[148:149], v[80:81] op_sel:[0,1,0] op_sel_hi:[1,1,1] neg_lo:[1,0,0] neg_hi:[1,0,0]\n"
    "v_pk_fma_f32 v[114:115], v[242:243], v[148:149], v[114:115] op_sel_hi:[1,0,1] neg_lo:[1,0,0] neg_hi:[1,0,0]\n"
    "v_pk_fma_f32 v[82:83], v[244:245], v[148:149], v[82:83] op_sel:[0,1,0] op_sel_hi:[1,1,1] neg_lo:[1,0,0] neg_hi:[1,0,0]\n"
    "v_pk_fma_f32 v[116:117], v[244:245], v[148:149], v[116:117] op_sel_hi:[1,0,1] neg_lo:[1,0,0] neg_hi:[1,0,0]\n"
    "v_pk_fma_f32 v[84:85], v[246:247], v[148:149], v[84:85] op_sel:[0,1,0] op_sel_hi:[1,1,1] neg_lo:[1,0,0] neg_hi:[1,0,0]\n"
    "v_pk_fma_f32 v[118:119], v[246:247], v[148:149], v[118:119] op_sel_hi:[1,0,1] neg_lo:[1,0,0] neg_hi:[1,0,0]\n"
    "v_cvt_pk_bf16_f32 v176, v78, v79\n"
    "v_cvt_pk_bf16_f32 v177, v80, v81\n"
    "v_cvt_pk_bf16_f32 v178, v82, v83\n"
    "v_cvt_pk_bf16_f32 v179, v84, v85\n"
    "buffer_store_dwordx4 v[176:179], v59, s[20:23], s5 offen offset:0 sc1\n"
    "v_cvt_pk_bf16_f32 v180, v112, v113\n"
    "v_cvt_pk_bf16_f32 v181, v114, v115\n"
    "v_cvt_pk_bf16_f32 v182, v116, v117\n"
    "v_cvt_pk_bf16_f32 v183, v118, v119\n"
    "buffer_store_dwordx4 v[180:183], v59, s[20:23], s5 offen offset:2048 sc1\n"
    "v_pk_fma_f32 v[86:87], v[216:217], v[148:149], v[86:87] op_sel_hi:[1,0,1]\n"
    "v_pk_fma_f32 v[120:121], v[216:217], v[148:149], v[120:121] op_sel:[0,1,0] op_sel_hi:[1,1,1] neg_lo:[1,0,0] neg_hi:[1,0,0]\n"
    "v_pk_fma_f32 v[88:89], v[218:219], v[148:149], v[88:89] op_sel_hi:[1,0,1]\n"
    "v_pk_fma_f32 v[122:123], v[218:219], v[148:149], v[122:123] op_sel:[0,1,0] op_sel_hi:[1,1,1] neg_lo:[1,0,0] neg_hi:[1,0,0]\n"
    "v_pk_fma_f32 v[90:91], v[220:221], v[148:149], v[90:91] op_sel_hi:[1,0,1]\n"
    "v_pk_fma_f32 v[124:125], v[220:221], v[148:149], v[124:125] op_sel:[0,1,0] op_sel_hi:[1,1,1] neg_lo:[1,0,0] neg_hi:[1,0,0]\n"
    "v_pk_fma_f32 v[92:93], v[222:223], v[148:149], v[92:93] op_sel_hi:[1,0,1]\n"
    "v_pk_fma_f32 v[126:127], v[222:223], v[148:149], v[126:127] op_sel:[0,1,0] op_sel_hi:[1,1,1] neg_lo:[1,0,0] neg_hi:[1,0,0]\n"
    "v_pk_fma_f32 v[86:87], v[248:249], v[148:149], v[86:87] op_sel:[0,1,0] op_sel_hi:[1,1,1] neg_lo:[1,0,0] neg_hi:[1,0,0]\n"
    "v_pk_fma_f32 v[120:121], v[248:249], v[148:149], v[120:121] op_sel_hi:[1,0,1] neg_lo:[1,0,0] neg_hi:[1,0,0]\n"
    "v_pk_fma_f32 v[88:89], v[250:251], v[148:149], v[88:89] op_sel:[0,1,0] op_sel_hi:[1,1,1] neg_lo:[1,0,0] neg_hi:[1,0,0]\n"
    "v_pk_fma_f32 v[122:123], v[250:251], v[148:149], v[122:123] op_sel_hi:[1,0,1] neg_lo:[1,0,0] neg_hi:[1,0,0]\n"
    "v_pk_fma_f32 v[90:91], v[252:253], v[148:149], v[90:91] op_sel:[0,1,0] op_sel_hi:[1,1,1] neg_lo:[1,0,0] neg_hi:[1,0,0]\n"
    "v_pk_fma_f32 v[124:125], v[252:253], v[148:149], v[124:125] op_sel_hi:[1,0,1] neg_lo:[1,0,0] neg_hi:[1,0,0]\n"
    "v_pk_fma_f32 v[92:93], v[254:255], v[148:149], v[92:93] op_sel:[0,1,0] op_sel_hi:[1,1,1] neg_lo:[1,0,0] neg_hi:[1,0,0]\n"
    "v_pk_fma_f32 v[126:127], v[254:255], v[148:149], v[126:127] op_sel_hi:[1,0,1] neg_lo:[1,0,0] neg_hi:[1,0,0]\n"
    "v_cvt_pk_bf16_f32 v184, v86, v87\n"
    "v_cvt_pk_bf16_f32 v185, v88, v89\n"
    "v_cvt_pk_bf16_f32 v186, v90, v91\n"
    "v_cvt_pk_bf16_f32 v187, v92, v93\n"
    "buffer_store_dwordx4 v[184:187], v56, s[20:23], s5 offen offset:1024 sc1\n"
    "v_cvt_pk_bf16_f32 v188, v120, v121\n"
    "v_cvt_pk_bf16_f32 v189, v122, v123\n"
    "v_cvt_pk_bf16_f32 v190, v124, v125\n"
    "v_cvt_pk_bf16_f32 v191, v126, v127\n"
    "buffer_store_dwordx4 v[188:191], v56, s[20:23], s5 offen offset:3072 sc1\n"
    "s_barrier\n"
    "v_mov_b32 v24, s80\n"
    "s_xor_b32 s84, s87, 5\n"
    "s_lshr_b32 s85, s84, 1\n"
    "s_lshl_b32 s85, s85, 8\n"
    "s_bitcmp1_b32 s84, 0\n"
    "s_cselect_b32 s12, s60, s58\n"
    "s_cselect_b32 s13, s61, s59\n"
    "s_add_u32 s12, s12, s85\n"
    "s_addc_u32 s13, s13, 0\n"
    "s_and_b32 s13, s13, 0xffff\n"
    "s_sub_u32 s14, 524288, s85\n"
    "s_add_u32 m0, s77, 32768\n"
    "s_mov_b32 s86, 65536\n"
    "buffer_load_dwordx4 v10, s[12:15], 0 offen lds\n"
    "v_mov_b32 v25, s81\n"
    "v_and_b32 v6, 31, v1\n"
    "v_lshrrev_b32 v7, 5, v1\n"
    "v_lshlrev_b32 v4, 3, v7\n"
    "ds_read_b128 a[64:67], v152 offset:0\n"
    "v_sub_u32 v4, v6, v4\n"
    "v_subrev_u32 v5, 0, v4\n"
    "v_lshlrev_b32 v8, 4, v5\n"
    "v_cmp_gt_u32 vcc, 2, v5\n"
    "v_lshlrev_b32 v8, v8, v63\n"
    "s_nop 0\n"
    "v_cndmask_b32 v30, 0, v8, vcc\n"
    "buffer_load_dwordx4 v11, s[12:15], 0 offen offset:1024 lds\n"
    "v_subrev_u32 v5, 2, v4\n"
    "v_lshlrev_b32 v8, 4, v5\n"
    "v_cmp_gt_u32 vcc, 2, v5\n"
    "v_lshlrev_b32 v8, v8, v63\n"
    "s_nop 0\n"
    "v_cndmask_b32 v31, 0, v8, vcc\n"
    "ds_read_b128 a[68:71], v153 offset:0\n"
    "v_subrev_u32 v5, 4, v4\n"
    "v_lshlrev_b32 v8, 4, v5\n"
    "v_cmp_gt_u32 vcc, 2, v5\n"
    "v_lshlrev_b32 v8, v8, v63\n"
    "s_nop 0\n"
    "v_cndmask_b32 v32, 0, v8, vcc\n"
    "buffer_load_dwordx4 v12, s[12:15], 0 offen offset:2048 lds\n"
    "v_subrev_u32 v5, 6, v4\n"
    "v_lshlrev_b32 v8, 4, v5\n"
    "v_cmp_gt_u32 vcc, 2, v5\n"
    "v_lshlrev_b32 v8, v8, v63\n"
    "s_nop 0\n"
    "v_cndmask_b32 v33, 0, v8, vcc\n"
    "ds_read_b128 a[72:75], v154 offset:0\n"
    "v_subrev_u32 v5, 16, v4\n"
    "v_lshlrev_b32 v8, 4, v5\n"
    "v_cmp_gt_u32 vcc, 2, v5\n"
    "v_lshlrev_b32 v8, v8, v63\n"
    "s_nop 0\n"
    "v_cndmask_b32 v34, 0, v8, vcc\n"
    "buffer_load_dwordx4 v13, s[12:15], 0 offen offset:3072 lds\n"
    "v_subrev_u32 v5, 18, v4\n"
    "v_lshlrev_b32 v8, 4, v5\n"
    "ds_read_b128 a[76:79], v155 offset:0\n"
    "v_cmp_gt_u32 vcc, 2, v5\n"
    "v_lshlrev_b32 v8, v8, v63\n"
    "s_nop 0\n"
    "v_cndmask_b32 v35, 0, v8, vcc\n"
    "v_subrev_u32 v5, 20, v4\n"
    "v_lshlrev_b32 v8, 4, v5\n"
    "s_add_u32 m0, m0, 0x4000\n"
    "s_nop 0\n"
    "buffer_load_dwordx4 v10, s[12:15], s86 offen offset:0 lds\n"
    "v_cmp_gt_u32 vcc, 2, v5\n"
    "v_lshlrev_b32 v8, v8, v63\n"
    "s_nop 0\n"
    "v_cndmask_b32 v36, 0, v8, vcc\n"
    "v_subrev_u32 v5, 22, v4\n"
    "v_lshlrev_b32 v8, 4, v5\n"
    "ds_read_b128 a[80:83], v156 offset:0\n"
    "v_cmp_gt_u32 vcc, 2, v5\n"
    "v_lshlrev_b32 v8, v8, v63\n"
    "s_nop 0\n"
    "v_cndmask_b32 v37, 0, v8, vcc\n"
    "s_and_b32 s72, s69, 1\n"
    "s_lshr_b32 s73, s69, 1\n"
    "s_and_b32 s76, s73, 1\n"
    "v_lshl_add_u32 v26, v1, 4, s77\n"
    "buffer_load_dwordx4 v11, s[12:15], s86 offen offset:1024 lds\n"
    "v_add_u32 v27, 16384, v26\n"
    "v_and_b32 v6, 31, v1\n"
    "ds_read_b128 a[84:87], v157 offset:0\n"
    "v_lshrrev_b32 v7, 5, v1\n"
    "v_bfe_u32 v2, v1, 2, 2\n"
    "v_bfe_u32 v3, v1, 4, 1\n"
    "v_bfe_u32 v4, v1, 1, 1\n"
    "v_and_b32 v5, 1, v1\n"
    "buffer_load_dwordx4 v12, s[12:15], s86 offen offset:2048 lds\n"
    "v_xor_b32 v9, v7, v3\n"
    "v_lshl_add_u32 v9, v9, 1, v4\n"
    "v_lshl_add_u32 v9, v9, 2, v2\n"
    "v_lshl_add_u32 v9, v3, 5, v9\n"
    "ds_read_b128 a[88:91], v158 offset:0\n"
    "v_lshlrev_b32 v9, 4, v9\n"
    "v_lshl_add_u32 v9, v5, 3, v9\n"
    "v_lshl_add_u32 v9, v4, 10, v9\n"
    "s_and_b32 s7, s87, 1\n"
    "s_lshl_b32 s7, s7, 11\n"
    "s_bfe_u32 s34, s87, 0x10001\n"
    "s_lshl_b32 s34, s34, 14\n"
    "s_add_u32 s7, s7, s34\n"
    "v_add_u32 v40, s7, v9\n"
    "buffer_load_dwordx4 v13, s[12:15], s86 offen offset:3072 lds\n"
    "v_xor_b32 v41, 64, v40\n"
    "ds_read_b128 a[92:95], v159 offset:0\n"
    "v_xor_b32 v42, 0x800, v40\n"
    "v_xor_b32 v43, 0x800, v41\n"
    "v_xor_b32 v44, 0x4000, v40\n"
    "v_xor_b32 v45, 0x4000, v41\n"
    "v_xor_b32 v46, 0x4000, v42\n"
    "ds_read_b128 a[96:99], v152 offset:32768\n"
    "v_xor_b32 v47, 0x4000, v43\n"
    "v_add_u32 v48, 0x10000, v40\n"
    "v_add_u32 v49, 0x10000, v41\n"
    "v_add_u32 v50, 0x10000, v42\n"
    "ds_read_b128 a[100:103], v153 offset:32768\n"
    "v_add_u32 v51, 0x10000, v43\n"
    "v_add_u32 v52, 0x10000, v44\n"
    "v_add_u32 v53, 0x10000, v45\n"
    "v_add_u32 v54, 0x10000, v46\n"
    "v_add_u32 v55, 0x10000, v47\n"
    "ds_read_b128 a[104:107], v154 offset:32768\n"
    "v_bfe_u32 v2, v1, 1, 3\n"
    "s_lshl_b32 s7, s67, 2\n"
    "v_xor_b32 v2, v2, v7\n"
    "v_xor_b32 v2, s7, v2\n"
    "v_lshlrev_b32 v2, 4, v2\n"
    "ds_read_b128 a[108:111], v155 offset:32768\n"
    "v_lshl_or_b32 v2, v6, 7, v2\n"
    "s_lshl_b32 s7, s66, 1\n"
    "s_add_u32 s7, s7, s76\n"
    "s_add_u32 s7, s7, 36\n"
    "s_lshl_b32 s7, s7, 12\n"
    "v_add_u32 v18, s7, v2\n"
    "ds_read_b128 a[112:115], v156 offset:32768\n"
    "v_xor_b32 v19, 32, v18\n"
    "v_xor_b32 v20, 0x1000, v18\n"
    "v_xor_b32 v21, 0x1000, v19\n"
    "s_lshl_b32 s7, s65, 7\n"
    "v_lshlrev_b32 v2, 14, v7\n"
    "ds_read_b128 a[116:119], v157 offset:32768\n"
    "v_lshl_add_u32 v2, v6, 2, v2\n"
    "v_add_u32 v136, s7, v2\n"
    "v_add_u32 v137, 4096, v136\n"
    "ds_read_b128 a[120:123], v158 offset:32768\n"
    "v_add_u32 v138, 8192, v136\n"
    "v_add_u32 v139, 12288, v136\n"
    "v_add_u32 v140, 32768, v136\n"
    "v_add_u32 v141, 36864, v136\n"
    "ds_read_b128 a[124:127], v159 offset:32768\n"
    "v_add_u32 v142, 40960, v136\n"
    "v_add_u32 v143, 45056, v136\n"
    "v_add_u32 v144, 131072, v136\n"
    "v_add_u32 v145, 135168, v136\n"
    "v_add_u32 v146, 139264, v136\n"
    "v_xor_b32 v152, 0x10000, v152\n"
    "v_add_u32 v147, 143360, v136\n"
    "v_add_u32 v148, 163840, v136\n"
    "v_add_u32 v149, 167936, v136\n"
    "v_add_u32 v150, 172032, v136\n"
    "v_add_u32 v151, 176128, v136\n"
    "s_lshl_b32 s34, s68, 1\n"
    "s_add_u32 s34, s34, s72\n"
    "s_lshl_b32 s34, s34, 18\n"
    "s_lshl_b32 s35, s73, 9\n"
    "s_add_u32 s35, s35, 2048\n"
    "s_add_u32 s34, s34, s35\n"
    "s_sub_u32 s7, 262144, s35\n"
    "s_add_u32 s24, s62, s34\n"
    "s_addc_u32 s25, s63, 0\n"
    "s_and_b32 s25, s25, 0xffff\n"
    "s_mov_b32 s26, s7\n"
    "s_mov_b32 s27, 0x20000\n"
    "s_lshl_b32 s34, s68, 2\n"
    "s_add_u32 s34, s34, s72\n"
    "s_lshl_b32 s34, s34, 16\n"
    "s_add_u32 s34, s34, 4194304\n"
    "s_add_u32 s8, s46, s34\n"
    "s_addc_u32 s9, s47, 0\n"
    "s_and_b32 s9, s9, 0xffff\n"
    "s_mov_b32 s10, 196608\n"
    "s_mov_b32 s11, 0x20000\n"
    "s_lshl_b32 s74, s76, 17\n"
    "s_xor_b32 s78, s74, 131072\n"
    "s_lshr_b32 s34, s87, 2\n"
    "s_lshl_b32 s34, s34, 15\n"
    "s_or_b32 s74, s74, s34\n"
    "s_or_b32 s78, s78, s34\n"
    "s_lshl_b32 s18, s76, 16\n"
    "s_xor_b32 s19, s18, 65536\n"
    "s_lshl_b32 s34, s68, 10\n"
    "s_add_u32 s34, s34, 655360\n"
    "v_and_b32 v28, 7, v1\n"
    "v_xor_b32 v153, 0x10000, v153\n"
    "v_xor_b32 v154, 0x10000, v154\n"
    "v_xor_b32 v155, 0x10000, v155\n"
    "v_xor_b32 v156, 0x10000, v156\n"
    "v_xor_b32 v157, 0x10000, v157\n"
    "v_xor_b32 v158, 0x10000, v158\n"
    "v_xor_b32 v159, 0x10000, v159\n"
    "v_lshl_add_u32 v28, v28, 7, s34\n"
    "s_waitcnt vmcnt(8) lgkmcnt(0)\n"
    "s_barrier\n"
    "buffer_store_dwordx2 v[24:25], off, s[20:23], s16 sc1\n"
    "s_xor_b32 s84, s87, 6\n"
    "s_lshr_b32 s85, s84, 1\n"
    "s_lshl_b32 s85, s85, 8\n"
    "s_bitcmp1_b32 s84, 0\n"
    "s_cselect_b32 s12, s60, s58\n"
    "s_cselect_b32 s13, s61, s59\n"
    "s_add_u32 s12, s12, s85\n"
    "s_addc_u32 s13, s13, 0\n"
    "s_and_b32 s13, s13, 0xffff\n"
    "s_sub_u32 s14, 524288, s85\n"
    "s_add_u32 m0, s77, 65536\n"
    "s_mov_b32 s86, 65536\n"
    "buffer_load_dwordx4 v10, s[12:15], 0 offen lds\n"
    "buffer_load_dwordx4 v11, s[12:15], 0 offen offset:1024 lds\n"
    "buffer_load_dwordx4 v12, s[12:15], 0 offen offset:2048 lds\n"
    "buffer_load_dwordx4 v13, s[12:15], 0 offen offset:3072 lds\n"
    "s_add_u32 m0, m0, 0x4000\n"
    "s_nop 0\n"
    "buffer_load_dwordx4 v10, s[12:15], s86 offen offset:0 lds\n"
    "buffer_load_dwordx4 v11, s[12:15], s86 offen offset:1024 lds\n"
    "buffer_load_dwordx4 v12, s[12:15], s86 offen offset:2048 lds\n"
    "buffer_load_dwordx4 v13, s[12:15], s86 offen offset:3072 lds\n"
    "s_xor_b32 s84, s87, 7\n"
    "s_lshr_b32 s85, s84, 1\n"
    "s_lshl_b32 s85, s85, 8\n"
    "s_bitcmp1_b32 s84, 0\n"
    "s_cselect_b32 s12, s60, s58\n"
    "s_cselect_b32 s13, s61, s59\n"
    "s_add_u32 s12, s12, s85\n"
    "s_addc_u32 s13, s13, 0\n"
    "s_and_b32 s13, s13, 0xffff\n"
    "s_sub_u32 s14, 524288, s85\n"
    "s_add_u32 m0, s77, 98304\n"
    "s_mov_b32 s86, 65536\n"
    "buffer_load_dwordx4 v10, s[12:15], 0 offen lds\n"
    "buffer_load_dwordx4 v11, s[12:15], 0 offen offset:1024 lds\n"
    "buffer_load_dwordx4 v12, s[12:15], 0 offen offset:2048 lds\n"
    "buffer_load_dwordx4 v13, s[12:15], 0 offen offset:3072 lds\n"
    "s_add_u32 m0, m0, 0x4000\n"
    "s_nop 0\n"
    "buffer_load_dwordx4 v10, s[12:15], s86 offen offset:0 lds\n"
    "buffer_load_dwordx4 v11, s[12:15], s86 offen offset:1024 lds\n"
    "buffer_load_dwordx4 v12, s[12:15], s86 offen offset:2048 lds\n"
    "buffer_load_dwordx4 v13, s[12:15], s86 offen offset:3072 lds\n"
    "s_waitcnt vmcnt(17)\n"
    "s_barrier\n"
    "ds_read_b128 a[128:131], v152 offset:0\n"
    "ds_read_b128 a[132:135], v153 offset:0\n"
    "ds_read_b128 a[136:139], v154 offset:0\n"
    "ds_read_b128 a[140:143], v155 offset:0\n"
    "ds_read_b128 a[144:147], v156 offset:0\n"
    "ds_read_b128 a[148:151], v157 offset:0\n"
    "ds_read_b128 a[152:155], v158 offset:0\n"
    "ds_read_b128 a[156:159], v159 offset:0\n"
    "ds_read_b128 a[160:163], v152 offset:32768\n"
    "ds_read_b128 a[164:167], v153 offset:32768\n"
    "ds_read_b128 a[168:171], v154 offset:32768\n"
    "ds_read_b128 a[172:175], v155 offset:32768\n"
    "ds_read_b128 a[176:179], v156 offset:32768\n"
    "ds_read_b128 a[180:183], v157 offset:32768\n"
    "ds_read_b128 a[184:187], v158 offset:32768\n"
    "ds_read_b128 a[188:191], v159 offset:32768\n"
    "v_xor_b32 v152, 0x10000, v152\n"
    "v_xor_b32 v153, 0x10000, v153\n"
    "v_xor_b32 v154, 0x10000, v154\n"
    "v_xor_b32 v155, 0x10000, v155\n"
    "v_xor_b32 v156, 0x10000, v156\n"
    "v_xor_b32 v157, 0x10000, v157\n"
    "v_xor_b32 v158, 0x10000, v158\n"
    "v_xor_b32 v159, 0x10000, v159\n"
    "s_waitcnt vmcnt(0)\n"
    "s_barrier\n"
    "ds_read_b128 a[192:195], v152 offset:0\n"
    "ds_read_b128 a[196:199], v153 offset:0\n"
    "ds_read_b128 a[200:203], v154 offset:0\n"
    "ds_read_b128 a[204:207], v155 offset:0\n"
    "ds_read_b128 a[208:211], v156 offset:0\n"
    "ds_read_b128 a[212:215], v157 offset:0\n"
    "ds_read_b128 a[216:219], v158 offset:0\n"
    "ds_read_b128 a[220:223], v159 offset:0\n"
    "ds_read_b128 a[224:227], v152 offset:32768\n"
    "ds_read_b128 a[228:231], v153 offset:32768\n"
    "ds_read_b128 a[232:235], v154 offset:32768\n"
    "ds_read_b128 a[236:239], v155 offset:32768\n"
    "ds_read_b128 a[240:243], v156 offset:32768\n"
    "ds_read_b128 a[244:247], v157 offset:32768\n"
    "ds_read_b128 a[248:251], v158 offset:32768\n"
    "ds_read_b128 a[252:255], v159 offset:32768\n"
    "s_waitcnt lgkmcnt(0)\n"
    "s_cmp_lg_u32 s65, 1\n"
    "s_cbranch_scc1 Lpw_6%=\n"
    "s_mov_b32 s79, 65536\n"
    "Lpoll_7%=:\n"
    "buffer_load_dwordx2 v[8:9], v28, s[20:23], 0 offen sc1\n"
    "s_sub_u32 s79, s79, 1\n"
    "s_cbranch_scc1 Lpdone_8%=\n"
    "s_waitcnt vmcnt(0)\n"
    "v_cmp_ne_u64 vcc, s[80:81], v[8:9]\n"
    "s_nop 0\n"
    "s_cbranch_vccnz Lpoll_7%=\n"
    "Lpdone_8%=:\n"
    "s_waitcnt vmcnt(0)\n"
    "Lpw_6%=:\n"
    "s_barrier\n"
    "s_add_u32 m0, s77, 0\n"
    "s_xor_b32 s4, s74, 0\n"
    "buffer_load_dwordx4 v26, s[8:11], s4 offen offset:0 sc1 lds\n"
    "buffer_load_dwordx4 v26, s[8:11], s4 offen offset:1024 sc1 lds\n"
    "buffer_load_dwordx4 v26, s[8:11], s4 offen offset:2048 sc1 lds\n"
    "buffer_load_dwordx4 v26, s[8:11], s4 offen offset:3072 sc1 lds\n"
    "s_add_u32 m0, m0, 0x4000\n"
    "s_nop 0\n"
    "buffer_load_dwordx4 v27, s[8:11], s4 offen offset:0 sc1 lds\n"
    "buffer_load_dwordx4 v27, s[8:11], s4 offen offset:1024 sc1 lds\n"
    "buffer_load_dwordx4 v27, s[8:11], s4 offen offset:2048 sc1 lds\n"
    "buffer_load_dwordx4 v27, s[8:11], s4 offen offset:3072 sc1 lds\n"
    "s_add_u32 m0, s77, 32768\n"
    "s_xor_b32 s4, s74, 32768\n"
    "buffer_load_dwordx4 v26, s[8:11], s4 offen offset:0 sc1 lds\n"
    "buffer_load_dwordx4 v26, s[8:11], s4 offen offset:1024 sc1 lds\n"
    "buffer_load_dwordx4 v26, s[8:11], s4 offen offset:2048 sc1 lds\n"
    "buffer_load_dwordx4 v26, s[8:11], s4 offen offset:3072 sc1 lds\n"
    "s_add_u32 m0, m0, 0x4000\n"
    "s_nop 0\n"
    "buffer_load_dwordx4 v27, s[8:11], s4 offen offset:0 sc1 lds\n"
    "buffer_load_dwordx4 v27, s[8:11], s4 offen offset:1024 sc1 lds\n"
    "buffer_load_dwordx4 v27, s[8:11], s4 offen offset:2048 sc1 lds\n"
    "buffer_load_dwordx4 v27, s[8:11], s4 offen offset:3072 sc1 lds\n"
    "s_or_b32 s4, s69, s65\n"
    "s_xor_b32 s5, s68, 15\n"
    "s_or_b32 s4, s4, s5\n"
    "s_cmp_lg_u32 s4, 0\n"
    "s_cbranch_scc1 Lnb_9%=\n"
    "v_mov_b32 v2, s80\n"
    "v_mov_b32 v3, s81\n"
    "s_movk_i32 s4, 256\n"
    "buffer_store_dwordx2 v[2:3], off, s[20:23], s4 sc1\n"
    "Lnb_9%=:\n"
    "s_waitcnt vmcnt(8)\n"
    "s_barrier\n"
    "ds_read_b128 v[128:131], v18\n"
    "ds_read_b128 v[132:135], v19\n"
    "ds_read_b64_tr_b16 v[96:97], v40 offset:0\n"
    "ds_read_b64_tr_b16 v[98:99], v41 offset:0\n"
    "ds_read_b64_tr_b16 v[100:101], v40 offset:256\n"
    "ds_read_b64_tr_b16 v[102:103], v41 offset:256\n"
    "ds_read_b64_tr_b16 v[104:105], v40 offset:4096\n"
    "ds_read_b64_tr_b16 v[106:107], v41 offset:4096\n"
    "ds_read_b64_tr_b16 v[108:109], v40 offset:4352\n"
    "ds_read_b64_tr_b16 v[110:111], v41 offset:4352\n"
    "ds_read_b64_tr_b16 v[112:113], v40 offset:8192\n"
    "ds_read_b64_tr_b16 v[114:115], v41 offset:8192\n"
    "ds_read_b64_tr_b16 v[116:117], v40 offset:8448\n"
    "ds_read_b64_tr_b16 v[118:119], v41 offset:8448\n"
    "s_waitcnt lgkmcnt(12)\n"
    "v_mfma_f32_32x32x16_bf16 v[64:79], v[128:131], v[30:33], 0\n"
    "v_mfma_f32_32x32x16_bf16 v[64:79], v[132:135], v[34:37], v[64:79]\n"
    "s_waitcnt lgkmcnt(8)\n"
    "v_mfma_f32_32x32x16_bf16 v[64:79], v[96:99], a[0:3], v[64:79]\n"
    "ds_read_b64_tr_b16 v[120:121], v40 offset:12288\n"
    "ds_read_b64_tr_b16 v[122:123], v41 offset:12288\n"
    "v_mfma_f32_32x32x16_bf16 v[64:79], v[100:103], a[4:7], v[64:79]\n"
    "ds_read_b64_tr_b16 v[124:125], v40 offset:12544\n"
    "ds_read_b64_tr_b16 v[126:127], v41 offset:12544\n"
    "s_waitcnt lgkmcnt(8)\n"
    "v_mfma_f32_32x32x16_bf16 v[64:79], v[104:107], a[8:11], v[64:79]\n"
    "ds_read_b64_tr_b16 v[96:97], v42 offset:0\n"
    "ds_read_b64_tr_b16 v[98:99], v43 offset:0\n"
    "s_add_u32 m0, s77, 65536\n"
    "s_xor_b32 s4, s78, 0\n"
    "buffer_load_dwordx4 v26, s[8:11], s4 offen offset:0 sc1 lds\n"
    "v_mfma_f32_32x32x16_bf16 v[64:79], v[108:111], a[12:15], v[64:79]\n"
    "ds_read_b64_tr_b16 v[100:101], v42 offset:256\n"
    "ds_read_b64_tr_b16 v[102:103], v43 offset:256\n"
    "s_waitcnt lgkmcnt(8)\n"
    "v_mfma_f32_32x32x16_bf16 v[64:79], v[112:115], a[16:19], v[64:79]\n"
    "ds_read_b64_tr_b16 v[104:105], v42 offset:4096\n"
    "ds_read_b64_tr_b16 v[106:107], v43 offset:4096\n"
    "v_mfma_f32_32x32x16_bf16 v[64:79], v[116:119], a[20:23], v[64:79]\n"
    "ds_read_b64_tr_b16 v[108:109], v42 offset:4352\n"
    "ds_read_b64_tr_b16 v[110:111], v43 offset:4352\n"
    "buffer_load_dwordx4 v26, s[8:11], s4 offen offset:1024 sc1 lds\n"
    "s_waitcnt lgkmcnt(8)\n"
    "v_mfma_f32_32x32x16_bf16 v[64:79], v[120:123], a[24:27], v[64:79]\n"
    "ds_read_b64_tr_b16 v[112:113], v42 offset:8192\n"
    "ds_read_b64_tr_b16 v[114:115], v43 offset:8192\n"
    "v_mfma_f32_32x32x16_bf16 v[64:79], v[124:127], a[28:31], v[64:79]\n"
    "ds_read_b64_tr_b16 v[116:117], v42 offset:8448\n"
    "ds_read_b64_tr_b16 v[118:119], v43 offset:8448\n"
    "s_waitcnt lgkmcnt(8)\n"
    "v_mfma_f32_32x32x16_bf16 v[64:79], v[96:99], a[32:35], v[64:79]\n"
    "ds_read_b64_tr_b16 v[120:121], v42 offset:12288\n"
    "ds_read_b64_tr_b16 v[122:123], v43 offset:12288\n"
    "buffer_load_dwordx4 v26, s[8:11], s4 offen offset:2048 sc1 lds\n"
    "v_mfma_f32_32x32x16_bf16 v[64:79], v[100:103], a[36:39], v[64:79]\n"
    "ds_read_b64_tr_b16 v[124:125], v42 offset:12544\n"
    "ds_read_b64_tr_b16 v[126:127], v43 offset:12544\n"
    "s_waitcnt lgkmcnt(8)\n"
    "v_mfma_f32_32x32x16_bf16 v[64:79], v[104:107], a[40:43], v[64:79]\n"
    "ds_read_b64_tr_b16 v[96:97], v44 offset:0\n"
    "ds_read_b64_tr_b16 v[98:99], v45 offset:0\n"
    "buffer_load_dwordx4 v26, s[8:11], s4 offen offset:3072 sc1 lds\n"
    "v_mfma_f32_32x32x16_bf16 v[64:79], v[108:111], a[44:47], v[64:79]\n"
    "ds_read_b64_tr_b16 v[100:101], v44 offset:256\n"
    "ds_read_b64_tr_b16 v[102:103], v45 offset:256\n"
    "s_waitcnt lgkmcnt(8)\n"
    "v_mfma_f32_32x32x16_bf16 v[64:79], v[112:115], a[48:51], v[64:79]\n"
    "ds_read_b64_tr_b16 v[104:105], v44 offset:4096\n"
    "ds_read_b64_tr_b16 v[106:107], v45 offset:4096\n"
    "v_mfma_f32_32x32x16_bf16 v[64:79], v[116:119], a[52:55], v[64:79]\n"
    "ds_read_b64_tr_b16 v[108:109], v44 offset:4352\n"
    "ds_read_b64_tr_b16 v[110:111], v45 offset:4352\n"
    "s_add_u32 m0, m0, 0x4000\n"
    "s_nop 0\n"
    "buffer_load_dwordx4 v27, s[8:11], s4 offen offset:0 sc1 lds\n"
    "s_waitcnt lgkmcnt(8)\n"
    "v_mfma_f32_32x32x16_bf16 v[64:79], v[120:123], a[56:59], v[64:79]\n"
    "ds_read_b64_tr_b16 v[112:113], v44 offset:8192\n"
    "ds_read_b64_tr_b16 v[114:115], v45 offset:8192\n"
    "v_mfma_f32_32x32x16_bf16 v[64:79], v[124:127], a[60:63], v[64:79]\n"
    "ds_read_b64_tr_b16 v[116:117], v44 offset:8448\n"
    "ds_read_b64_tr_b16 v[118:119], v45 offset:8448\n"
    "s_waitcnt lgkmcnt(8)\n"
    "v_mfma_f32_32x32x16_bf16 v[64:79], v[96:99], a[64:67], v[64:79]\n"
    "ds_read_b64_tr_b16 v[120:121], v44 offset:12288\n"
    "ds_read_b64_tr_b16 v[122:123], v45 offset:12288\n"
    "buffer_load_dwordx4 v27, s[8:11], s4 offen offset:1024 sc1 lds\n"
    "v_mfma_f32_32x32x16_bf16 v[64:79], v[100:103], a[68:71], v[64:79]\n"
    "ds_read_b64_tr_b16 v[124:125], v44 offset:12544\n"
    "ds_read_b64_tr_b16 v[126:127], v45 offset:12544\n"
    "s_waitcnt lgkmcnt(8)\n"
    "v_mfma_f32_32x32x16_bf16 v[64:79], v[104:107], a[72:75], v[64:79]\n"
    "ds_read_b64_tr_b16 v[96:97], v46 offset:0\n"
    "ds_read_b64_tr_b16 v[98:99], v47 offset:0\n"
    "v_mfma_f32_32x32x16_bf16 v[64:79], v[108:111], a[76:79], v[64:79]\n"
    "ds_read_b64_tr_b16 v[100:101], v46 offset:256\n"
    "ds_read_b64_tr_b16 v[102:103], v47 offset:256\n"
    "buffer_load_dwordx4 v27, s[8:11], s4 offen offset:2048 sc1 lds\n"
    "s_waitcnt lgkmcnt(8)\n"
    "v_mfma_f32_32x32x16_bf16 v[64:79], v[112:115], a[80:83], v[64:79]\n"
    "ds_read_b64_tr_b16 v[104:105], v46 offset:4096\n"
    "ds_read_b64_tr_b16 v[106:107], v47 offset:4096\n"
    "v_mfma_f32_32x32x16_bf16 v[64:79], v[116:119], a[84:87], v[64:79]\n"
    "ds_read_b64_tr_b16 v[108:109], v46 offset:4352\n"
    "ds_read_b64_tr_b16 v[110:111], v47 offset:4352\n"
    "buffer_load_dwordx4 v27, s[8:11], s4 offen offset:3072 sc1 lds\n"
    "s_waitcnt lgkmcnt(8)\n"
    "v_mfma_f32_32x32x16_bf16 v[64:79], v[120:123], a[88:91], v[64:79]\n"
    "ds_read_b64_tr_b16 v[112:113], v46 offset:8192\n"
    "ds_read_b64_tr_b16 v[114:115], v47 offset:8192\n"
    "v_mfma_f32_32x32x16_bf16 v[64:79], v[124:127], a[92:95], v[64:79]\n"
    "ds_read_b64_tr_b16 v[116:117], v46 offset:8448\n"
    "ds_read_b64_tr_b16 v[118:119], v47 offset:8448\n"
    "s_waitcnt lgkmcnt(8)\n"
    "v_mfma_f32_32x32x16_bf16 v[64:79], v[96:99], a[96:99], v[64:79]\n"
    "ds_read_b64_tr_b16 v[120:121], v46 offset:12288\n"
    "ds_read_b64_tr_b16 v[122:123], v47 offset:12288\n"
    "s_add_u32 m0, s77, 98304\n"
    "s_xor_b32 s4, s78, 32768\n"
    "buffer_load_dwordx4 v26, s[8:11], s4 offen offset:0 sc1 lds\n"
    "v_mfma_f32_32x32x16_bf16 v[64:79], v[100:103], a[100:103], v[64:79]\n"
    "ds_read_b64_tr_b16 v[124:125], v46 offset:12544\n"
    "ds_read_b64_tr_b16 v[126:127], v47 offset:12544\n"
    "s_waitcnt vmcnt(9)\n"
    "s_barrier\n"
    "s_waitcnt lgkmcnt(8)\n"
    "v_mfma_f32_32x32x16_bf16 v[64:79], v[104:107], a[104:107], v[64:79]\n"
    "ds_read_b64_tr_b16 v[96:97], v40 offset:32768\n"
    "ds_read_b64_tr_b16 v[98:99], v41 offset:32768\n"
    "v_mfma_f32_32x32x16_bf16 v[64:79], v[108:111], a[108:111], v[64:79]\n"
    "ds_read_b64_tr_b16 v[100:101], v40 offset:33024\n"
    "ds_read_b64_tr_b16 v[102:103], v41 offset:33024\n"
    "buffer_load_dwordx4 v26, s[8:11], s4 offen offset:1024 sc1 lds\n"
    "s_waitcnt lgkmcnt(8)\n"
    "v_mfma_f32_32x32x16_bf16 v[64:79], v[112:115], a[112:115], v[64:79]\n"
    "ds_read_b64_tr_b16 v[104:105], v40 offset:36864\n"
    "ds_read_b64_tr_b16 v[106:107], v41 offset:36864\n"
    "v_mfma_f32_32x32x16_bf16 v[64:79], v[116:119], a[116:119], v[64:79]\n"
    "ds_read_b64_tr_b16 v[108:109], v40 offset:37120\n"
    "ds_read_b64_tr_b16 v[110:111], v41 offset:37120\n"
    "s_waitcnt lgkmcnt(8)\n"
    "v_mfma_f32_32x32x16_bf16 v[64:79], v[120:123], a[120:123], v[64:79]\n"
    "ds_read_b64_tr_b16 v[112:113], v40 offset:40960\n"
    "ds_read_b64_tr_b16 v[114:115], v41 offset:40960\n"
    "buffer_load_dwordx4 v26, s[8:11], s4 offen offset:2048 sc1 lds\n"
    "v_mfma_f32_32x32x16_bf16 v[64:79], v[124:127], a[124:127], v[64:79]\n"
    "ds_read_b64_tr_b16 v[116:117], v40 offset:41216\n"
    "ds_read_b64_tr_b16 v[118:119], v41 offset:41216\n"
    "s_waitcnt lgkmcnt(8)\n"
    "v_mfma_f32_32x32x16_bf16 v[64:79], v[96:99], a[128:131], v[64:79]\n"
    "ds_read_b64_tr_b16 v[120:121], v40 offset:45056\n"
    "ds_read_b64_tr_b16 v[122:123], v41 offset:45056\n"
    "buffer_load_dwordx4 v26, s[8:11], s4 offen offset:3072 sc1 lds\n"
    "v_mfma_f32_32x32x16_bf16 v[64:79], v[100:103], a[132:135], v[64:79]\n"
    "ds_read_b64_tr_b16 v[124:125], v40 offset:45312\n"
    "ds_read_b64_tr_b16 v[126:127], v41 offset:45312\n"
    "s_waitcnt lgkmcnt(8)\n"
    "v_mfma_f32_32x32x16_bf16 v[64:79], v[104:107], a[136:139], v[64:79]\n"
    "ds_read_b64_tr_b16 v[96:97], v42 offset:32768\n"
    "ds_read_b64_tr_b16 v[98:99], v43 offset:32768\n"
    "v_mfma_f32_32x32x16_bf16 v[64:79], v[108:111], a[140:143], v[64:79]\n"
    "ds_read_b64_tr_b16 v[100:101], v42 offset:33024\n"
    "ds_read_b64_tr_b16 v[102:103], v43 offset:33024\n"
    "s_add_u32 m0, m0, 0x4000\n"
    "s_nop 0\n"
    "buffer_load_dwordx4 v27, s[8:11], s4 offen offset:0 sc1 lds\n"
    "s_waitcnt lgkmcnt(8)\n"
    "v_mfma_f32_32x32x16_bf16 v[64:79], v[112:115], a[144:147], v[64:79]\n"
    "ds_read_b64_tr_b16 v[104:105], v42 offset:36864\n"
    "ds_read_b64_tr_b16 v[106:107], v43 offset:36864\n"
    "v_mfma_f32_32x32x16_bf16 v[64:79], v[116:119], a[148:151], v[64:79]\n"
    "ds_read_b64_tr_b16 v[108:109], v42 offset:37120\n"
    "ds_read_b64_tr_b16 v[110:111], v43 offset:37120\n"
    "s_waitcnt lgkmcnt(8)\n"
    "v_mfma_f32_32x32x16_bf16 v[64:79], v[120:123], a[152:155], v[64:79]\n"
    "ds_read_b64_tr_b16 v[112:113], v42 offset:40960\n"
    "ds_read_b64_tr_b16 v[114:115], v43 offset:40960\n"
    "buffer_load_dwordx4 v27, s[8:11], s4 offen offset:1024 sc1 lds\n"
    "v_mfma_f32_32x32x16_bf16 v[64:79], v[124:127], a[156:159], v[64:79]\n"
    "ds_read_b64_tr_b16 v[116:117], v42 offset:41216\n"
    "ds_read_b64_tr_b16 v[118:119], v43 offset:41216\n"
    "s_waitcnt lgkmcnt(8)\n"
    "v_mfma_f32_32x32x16_bf16 v[64:79], v[96:99], a[160:163], v[64:79]\n"
    "ds_read_b64_tr_b16 v[120:121], v42 offset:45056\n"
    "ds_read_b64_tr_b16 v[122:123], v43 offset:45056\n"
    "v_mfma_f32_32x32x16_bf16 v[64:79], v[100:103], a[164:167], v[64:79]\n"
    "ds_read_b64_tr_b16 v[124:125], v42 offset:45312\n"
    "ds_read_b64_tr_b16 v[126:127], v43 offset:45312\n"
    "buffer_load_dwordx4 v27, s[8:11], s4 offen offset:2048 sc1 lds\n"
    "s_waitcnt lgkmcnt(8)\n"
    "v_mfma_f32_32x32x16_bf16 v[64:79], v[104:107], a[168:171], v[64:79]\n"
    "ds_read_b64_tr_b16 v[96:97], v44 offset:32768\n"
    "ds_read_b64_tr_b16 v[98:99], v45 offset:32768\n"
    "v_mfma_f32_32x32x16_bf16 v[64:79], v[108:111], a[172:175], v[64:79]\n"
    "ds_read_b64_tr_b16 v[100:101], v44 offset:33024\n"
    "ds_read_b64_tr_b16 v[102:103], v45 offset:33024\n"
    "buffer_load_dwordx4 v27, s[8:11], s4 offen offset:3072 sc1 lds\n"
    "s_waitcnt lgkmcnt(8)\n"
    "v_mfma_f32_32x32x16_bf16 v[64:79], v[112:115], a[176:179], v[64:79]\n"
    "ds_read_b64_tr_b16 v[104:105], v44 offset:36864\n"
    "ds_read_b64_tr_b16 v[106:107], v45 offset:36864\n"
    "v_mfma_f32_32x32x16_bf16 v[64:79], v[116:119], a[180:183], v[64:79]\n"
    "ds_read_b64_tr_b16 v[108:109], v44 offset:37120\n"
    "ds_read_b64_tr_b16 v[110:111], v45 offset:37120\n"
    "s_waitcnt lgkmcnt(8)\n"
    "v_mfma_f32_32x32x16_bf16 v[64:79], v[120:123], a[184:187], v[64:79]\n"
    "ds_read_b64_tr_b16 v[112:113], v44 offset:40960\n"
    "ds_read_b64_tr_b16 v[114:115], v45 offset:40960\n"
    "v_mfma_f32_32x32x16_bf16 v[64:79], v[124:127], a[188:191], v[64:79]\n"
    "ds_read_b64_tr_b16 v[116:117], v44 offset:41216\n"
    "ds_read_b64_tr_b16 v[118:119], v45 offset:41216\n"
    "s_waitcnt lgkmcnt(8)\n"
    "v_mfma_f32_32x32x16_bf16 v[64:79], v[96:99], a[192:195], v[64:79]\n"
    "ds_read_b64_tr_b16 v[120:121], v44 offset:45056\n"
    "ds_read_b64_tr_b16 v[122:123], v45 offset:45056\n"
    "v_mfma_f32_32x32x16_bf16 v[64:79], v[100:103], a[196:199], v[64:79]\n"
    "ds_read_b64_tr_b16 v[124:125], v44 offset:45312\n"
    "ds_read_b64_tr_b16 v[126:127], v45 offset:45312\n"
    "s_waitcnt lgkmcnt(8)\n"
    "v_mfma_f32_32x32x16_bf16 v[64:79], v[104:107], a[200:203], v[64:79]\n"
    "ds_read_b64_tr_b16 v[96:97], v46 offset:32768\n"
    "ds_read_b64_tr_b16 v[98:99], v47 offset:32768\n"
    "v_mfma_f32_32x32x16_bf16 v[64:79], v[108:111], a[204:207], v[64:79]\n"
    "ds_read_b64_tr_b16 v[100:101], v46 offset:33024\n"
    "ds_read_b64_tr_b16 v[102:103], v47 offset:33024\n"
    "s_waitcnt lgkmcnt(8)\n"
    "v_mfma_f32_32x32x16_bf16 v[64:79], v[112:115], a[208:211], v[64:79]\n"
    "ds_read_b64_tr_b16 v[104:105], v46 offset:36864\n"
    "ds_read_b64_tr_b16 v[106:107], v47 offset:36864\n"
    "v_mfma_f32_32x32x16_bf16 v[64:79], v[116:119], a[212:215], v[64:79]\n"
    "ds_read_b64_tr_b16 v[108:109], v46 offset:37120\n"
    "ds_read_b64_tr_b16 v[110:111], v47 offset:37120\n"
    "s_waitcnt lgkmcnt(8)\n"
    "v_mfma_f32_32x32x16_bf16 v[64:79], v[120:123], a[216:219], v[64:79]\n"
    "ds_read_b64_tr_b16 v[112:113], v46 offset:40960\n"
    "ds_read_b64_tr_b16 v[114:115], v47 offset:40960\n"
    "v_mfma_f32_32x32x16_bf16 v[64:79], v[124:127], a[220:223], v[64:79]\n"
    "ds_read_b64_tr_b16 v[116:117], v46 offset:41216\n"
    "ds_read_b64_tr_b16 v[118:119], v47 offset:41216\n"
    "s_waitcnt lgkmcnt(8)\n"
    "v_mfma_f32_32x32x16_bf16 v[64:79], v[96:99], a[224:227], v[64:79]\n"
    "ds_read_b64_tr_b16 v[120:121], v46 offset:45056\n"
    "ds_read_b64_tr_b16 v[122:123], v47 offset:45056\n"
    "v_mfma_f32_32x32x16_bf16 v[64:79], v[100:103], a[228:231], v[64:79]\n"
    "ds_read_b64_tr_b16 v[124:125], v46 offset:45312\n"
    "ds_read_b64_tr_b16 v[126:127], v47 offset:45312\n"
    "s_waitcnt vmcnt(8)\n"
    "s_barrier\n"
    "s_waitcnt lgkmcnt(8)\n"
    "v_mfma_f32_32x32x16_bf16 v[64:79], v[104:107], a[232:235], v[64:79]\n"
    "ds_read_b64_tr_b16 v[96:97], v48 offset:0\n"
    "ds_read_b64_tr_b16 v[98:99], v49 offset:0\n"
    "v_mfma_f32_32x32x16_bf16 v[64:79], v[108:111], a[236:239], v[64:79]\n"
    "ds_read_b64_tr_b16 v[100:101], v48 offset:256\n"
    "ds_read_b64_tr_b16 v[102:103], v49 offset:256\n"
    "s_waitcnt lgkmcnt(8)\n"
    "v_mfma_f32_32x32x16_bf16 v[64:79], v[112:115], a[240:243], v[64:79]\n"
    "ds_read_b64_tr_b16 v[104:105], v48 offset:4096\n"
    "ds_read_b64_tr_b16 v[106:107], v49 offset:4096\n"
    "v_mfma_f32_32x32x16_bf16 v[64:79], v[116:119], a[244:247], v[64:79]\n"
    "ds_read_b128 v[128:131], v20\n"
    "ds_read_b128 v[132:135], v21\n"
    "ds_read_b64_tr_b16 v[108:109], v48 offset:4352\n"
    "ds_read_b64_tr_b16 v[110:111], v49 offset:4352\n"
    "s_waitcnt lgkmcnt(10)\n"
    "v_mfma_f32_32x32x16_bf16 v[64:79], v[120:123], a[248:251], v[64:79]\n"
    "ds_read_b64_tr_b16 v[112:113], v48 offset:8192\n"
    "ds_read_b64_tr_b16 v[114:115], v49 offset:8192\n"
    "v_mfma_f32_32x32x16_bf16 v[64:79], v[124:127], a[252:255], v[64:79]\n"
    "ds_read_b64_tr_b16 v[116:117], v48 offset:8448\n"
    "ds_read_b64_tr_b16 v[118:119], v49 offset:8448\n"
    "s_waitcnt lgkmcnt(6)\n"
    "v_mfma_f32_32x32x16_bf16 v[80:95], v[128:131], v[30:33], 0\n"
    "v_mfma_f32_32x32x16_bf16 v[80:95], v[132:135], v[34:37], v[80:95]\n"
    "v_mfma_f32_32x32x16_bf16 v[80:95], v[96:99], a[0:3], v[80:95]\n"
    "ds_read_b64_tr_b16 v[120:121], v48 offset:12288\n"
    "ds_read_b64_tr_b16 v[122:123], v49 offset:12288\n"
    "v_mfma_f32_32x32x16_bf16 v[80:95], v[100:103], a[4:7], v[80:95]\n"
    "ds_read_b64_tr_b16 v[124:125], v48 offset:12544\n"
    "ds_read_b64_tr_b16 v[126:127], v49 offset:12544\n"
    "s_waitcnt lgkmcnt(8)\n"
    "v_mfma_f32_32x32x16_bf16 v[80:95], v[104:107], a[8:11], v[80:95]\n"
    "ds_read_b64_tr_b16 v[96:97], v50 offset:0\n"
    "ds_read_b64_tr_b16 v[98:99], v51 offset:0\n"
    "buffer_store_dword v64, v136, s[24:27], s18 offen sc1\n"
    "v_mfma_f32_32x32x16_bf16 v[80:95], v[108:111], a[12:15], v[80:95]\n"
    "ds_read_b64_tr_b16 v[100:101], v50 offset:256\n"
    "ds_read_b64_tr_b16 v[102:103], v51 offset:256\n"
    "buffer_store_dword v65, v137, s[24:27], s18 offen sc1\n"
    "s_waitcnt lgkmcnt(8)\n"
    "v_mfma_f32_32x32x16_bf16 v[80:95], v[112:115], a[16:19], v[80:95]\n"
    "ds_read_b64_tr_b16 v[104:105], v50 offset:4096\n"
    "ds_read_b64_tr_b16 v[106:107], v51 offset:4096\n"
    "buffer_store_dword v66, v138, s[24:27], s18 offen sc1\n"
    "v_mfma_f32_32x32x16_bf16 v[80:95], v[116:119], a[20:23], v[80:95]\n"
    "ds_read_b64_tr_b16 v[108:109], v50 offset:4352\n"
    "ds_read_b64_tr_b16 v[110:111], v51 offset:4352\n"
    "buffer_store_dword v67, v139, s[24:27], s18 offen sc1\n"
    "s_waitcnt lgkmcnt(8)\n"
    "v_mfma_f32_32x32x16_bf16 v[80:95], v[120:123], a[24:27], v[80:95]\n"
    "ds_read_b64_tr_b16 v[112:113], v50 offset:8192\n"
    "ds_read_b64_tr_b16 v[114:115], v51 offset:8192\n"
    "buffer_store_dword v68, v140, s[24:27], s18 offen sc1\n"
    "v_mfma_f32_32x32x16_bf16 v[80:95], v[124:127], a[28:31], v[80:95]\n"
    "ds_read_b64_tr_b16 v[116:117], v50 offset:8448\n"
    "ds_read_b64_tr_b16 v[118:119], v51 offset:8448\n"
    "buffer_store_dword v69, v141, s[24:27], s18 offen sc1\n"
    "s_waitcnt lgkmcnt(8)\n"
    "v_mfma_f32_32x32x16_bf16 v[80:95], v[96:99], a[32:35], v[80:95]\n"
    "ds_read_b64_tr_b16 v[120:121], v50 offset:12288\n"
    "ds_read_b64_tr_b16 v[122:123], v51 offset:12288\n"
    "buffer_store_dword v70, v142, s[24:27], s18 offen sc1\n"
    "v_mfma_f32_32x32x16_bf16 v[80:95], v[100:103], a[36:39], v[80:95]\n"
    "ds_read_b64_tr_b16 v[124:125], v50 offset:12544\n"
    "ds_read_b64_tr_b16 v[126:127], v51 offset:12544\n"
    "buffer_store_dword v71, v143, s[24:27], s18 offen sc1\n"
    "s_waitcnt lgkmcnt(8)\n"
    "v_mfma_f32_32x32x16_bf16 v[80:95], v[104:107], a[40:43], v[80:95]\n"
    "ds_read_b64_tr_b16 v[96:97], v52 offset:0\n"
    "ds_read_b64_tr_b16 v[98:99], v53 offset:0\n"
    "buffer_store_dword v72, v144, s[24:27], s18 offen sc1\n"
    "v_mfma_f32_32x32x16_bf16 v[80:95], v[108:111], a[44:47], v[80:95]\n"
    "ds_read_b64_tr_b16 v[100:101], v52 offset:256\n"
    "ds_read_b64_tr_b16 v[102:103], v53 offset:256\n"
    "buffer_store_dword v73, v145, s[24:27], s18 offen sc1\n"
    "s_waitcnt lgkmcnt(8)\n"
    "v_mfma_f32_32x32x16_bf16 v[80:95], v[112:115], a[48:51], v[80:95]\n"
    "ds_read_b64_tr_b16 v[104:105], v52 offset:4096\n"
    "ds_read_b64_tr_b16 v[106:107], v53 offset:4096\n"
    "buffer_store_dword v74, v146, s[24:27], s18 offen sc1\n"
    "v_mfma_f32_32x32x16_bf16 v[80:95], v[116:119], a[52:55], v[80:95]\n"
    "ds_read_b64_tr_b16 v[108:109], v52 offset:4352\n"
    "ds_read_b64_tr_b16 v[110:111], v53 offset:4352\n"
    "buffer_store_dword v75, v147, s[24:27], s18 offen sc1\n"
    "s_waitcnt lgkmcnt(8)\n"
    "v_mfma_f32_32x32x16_bf16 v[80:95], v[120:123], a[56:59], v[80:95]\n"
    "ds_read_b64_tr_b16 v[112:113], v52 offset:8192\n"
    "ds_read_b64_tr_b16 v[114:115], v53 offset:8192\n"
    "buffer_store_dword v76, v148, s[24:27], s18 offen sc1\n"
    "v_mfma_f32_32x32x16_bf16 v[80:95], v[124:127], a[60:63], v[80:95]\n"
    "ds_read_b64_tr_b16 v[116:117], v52 offset:8448\n"
    "ds_read_b64_tr_b16 v[118:119], v53 offset:8448\n"
    "buffer_store_dword v77, v149, s[24:27], s18 offen sc1\n"
    "s_waitcnt lgkmcnt(8)\n"
    "v_mfma_f32_32x32x16_bf16 v[80:95], v[96:99], a[64:67], v[80:95]\n"
    "ds_read_b64_tr_b16 v[120:121], v52 offset:12288\n"
    "ds_read_b64_tr_b16 v[122:123], v53 offset:12288\n"
    "buffer_store_dword v78, v150, s[24:27], s18 offen sc1\n"
    "v_mfma_f32_32x32x16_bf16 v[80:95], v[100:103], a[68:71], v[80:95]\n"
    "ds_read_b64_tr_b16 v[124:125], v52 offset:12544\n"
    "ds_read_b64_tr_b16 v[126:127], v53 offset:12544\n"
    "buffer_store_dword v79, v151, s[24:27], s18 offen sc1\n"
    "s_waitcnt lgkmcnt(8)\n"
    "v_mfma_f32_32x32x16_bf16 v[80:95], v[104:107], a[72:75], v[80:95]\n"
    "ds_read_b64_tr_b16 v[96:97], v54 offset:0\n"
    "ds_read_b64_tr_b16 v[98:99], v55 offset:0\n"
    "v_mfma_f32_32x32x16_bf16 v[80:95], v[108:111], a[76:79], v[80:95]\n"
    "ds_read_b64_tr_b16 v[100:101], v54 offset:256\n"
    "ds_read_b64_tr_b16 v[102:103], v55 offset:256\n"
    "s_waitcnt lgkmcnt(8)\n"
    "v_mfma_f32_32x32x16_bf16 v[80:95], v[112:115], a[80:83], v[80:95]\n"
    "ds_read_b64_tr_b16 v[104:105], v54 offset:4096\n"
    "ds_read_b64_tr_b16 v[106:107], v55 offset:4096\n"
    "v_mfma_f32_32x32x16_bf16 v[80:95], v[116:119], a[84:87], v[80:95]\n"
    "ds_read_b64_tr_b16 v[108:109], v54 offset:4352\n"
    "ds_read_b64_tr_b16 v[110:111], v55 offset:4352\n"
    "s_waitcnt lgkmcnt(8)\n"
    "v_mfma_f32_32x32x16_bf16 v[80:95], v[120:123], a[88:91], v[80:95]\n"
    "ds_read_b64_tr_b16 v[112:113], v54 offset:8192\n"
    "ds_read_b64_tr_b16 v[114:115], v55 offset:8192\n"
    "v_mfma_f32_32x32x16_bf16 v[80:95], v[124:127], a[92:95], v[80:95]\n"
    "ds_read_b64_tr_b16 v[116:117], v54 offset:8448\n"
    "ds_read_b64_tr_b16 v[118:119], v55 offset:8448\n"
    "s_waitcnt lgkmcnt(8)\n"
    "v_mfma_f32_32x32x16_bf16 v[80:95], v[96:99], a[96:99], v[80:95]\n"
    "ds_read_b64_tr_b16 v[120:121], v54 offset:12288\n"
    "ds_read_b64_tr_b16 v[122:123], v55 offset:12288\n"
    "v_mfma_f32_32x32x16_bf16 v[80:95], v[100:103], a[100:103], v[80:95]\n"
    "ds_read_b64_tr_b16 v[124:125], v54 offset:12544\n"
    "ds_read_b64_tr_b16 v[126:127], v55 offset:12544\n"
    "s_waitcnt vmcnt(16)\n"
    "s_barrier\n"
    "s_waitcnt lgkmcnt(8)\n"
    "v_mfma_f32_32x32x16_bf16 v[80:95], v[104:107], a[104:107], v[80:95]\n"
    "ds_read_b64_tr_b16 v[96:97], v48 offset:32768\n"
    "ds_read_b64_tr_b16 v[98:99], v49 offset:32768\n"
    "v_mfma_f32_32x32x16_bf16 v[80:95], v[108:111], a[108:111], v[80:95]\n"
    "ds_read_b64_tr_b16 v[100:101], v48 offset:33024\n"
    "ds_read_b64_tr_b16 v[102:103], v49 offset:33024\n"
    "s_waitcnt lgkmcnt(8)\n"
    "v_mfma_f32_32x32x16_bf16 v[80:95], v[112:115], a[112:115], v[80:95]\n"
    "ds_read_b64_tr_b16 v[104:105], v48 offset:36864\n"
    "ds_read_b64_tr_b16 v[106:107], v49 offset:36864\n"
    "v_mfma_f32_32x32x16_bf16 v[80:95], v[116:119], a[116:119], v[80:95]\n"
    "ds_read_b64_tr_b16 v[108:109], v48 offset:37120\n"
    "ds_read_b64_tr_b16 v[110:111], v49 offset:37120\n"
    "s_waitcnt lgkmcnt(8)\n"
    "v_mfma_f32_32x32x16_bf16 v[80:95], v[120:123], a[120:123], v[80:95]\n"
    "ds_read_b64_tr_b16 v[112:113], v48 offset:40960\n"
    "ds_read_b64_tr_b16 v[114:115], v49 offset:40960\n"
    "v_mfma_f32_32x32x16_bf16 v[80:95], v[124:127], a[124:127], v[80:95]\n"
    "ds_read_b64_tr_b16 v[116:117], v48 offset:41216\n"
    "ds_read_b64_tr_b16 v[118:119], v49 offset:41216\n"
    "s_waitcnt lgkmcnt(8)\n"
    "v_mfma_f32_32x32x16_bf16 v[80:95], v[96:99], a[128:131], v[80:95]\n"
    "ds_read_b64_tr_b16 v[120:121], v48 offset:45056\n"
    "ds_read_b64_tr_b16 v[122:123], v49 offset:45056\n"
    "v_mfma_f32_32x32x16_bf16 v[80:95], v[100:103], a[132:135], v[80:95]\n"
    "ds_read_b64_tr_b16 v[124:125], v48 offset:45312\n"
    "ds_read_b64_tr_b16 v[126:127], v49 offset:45312\n"
    "s_waitcnt lgkmcnt(8)\n"
    "v_mfma_f32_32x32x16_bf16 v[80:95], v[104:107], a[136:139], v[80:95]\n"
    "ds_read_b64_tr_b16 v[96:97], v50 offset:32768\n"
    "ds_read_b64_tr_b16 v[98:99], v51 offset:32768\n"
    "v_mfma_f32_32x32x16_bf16 v[80:95], v[108:111], a[140:143], v[80:95]\n"
    "ds_read_b64_tr_b16 v[100:101], v50 offset:33024\n"
    "ds_read_b64_tr_b16 v[102:103], v51 offset:33024\n"
    "s_waitcnt lgkmcnt(8)\n"
    "v_mfma_f32_32x32x16_bf16 v[80:95], v[112:115], a[144:147], v[80:95]\n"
    "ds_read_b64_tr_b16 v[104:105], v50 offset:36864\n"
    "ds_read_b64_tr_b16 v[106:107], v51 offset:36864\n"
    "v_mfma_f32_32x32x16_bf16 v[80:95], v[116:119], a[148:151], v[80:95]\n"
    "ds_read_b64_tr_b16 v[108:109], v50 offset:37120\n"
    "ds_read_b64_tr_b16 v[110:111], v51 offset:37120\n"
    "s_waitcnt lgkmcnt(8)\n"
    "v_mfma_f32_32x32x16_bf16 v[80:95], v[120:123], a[152:155], v[80:95]\n"
    "ds_read_b64_tr_b16 v[112:113], v50 offset:40960\n"
    "ds_read_b64_tr_b16 v[114:115], v51 offset:40960\n"
    "v_mfma_f32_32x32x16_bf16 v[80:95], v[124:127], a[156:159], v[80:95]\n"
    "ds_read_b64_tr_b16 v[116:117], v50 offset:41216\n"
    "ds_read_b64_tr_b16 v[118:119], v51 offset:41216\n"
    "s_waitcnt lgkmcnt(8)\n"
    "v_mfma_f32_32x32x16_bf16 v[80:95], v[96:99], a[160:163], v[80:95]\n"
    "ds_read_b64_tr_b16 v[120:121], v50 offset:45056\n"
    "ds_read_b64_tr_b16 v[122:123], v51 offset:45056\n"
    "v_mfma_f32_32x32x16_bf16 v[80:95], v[100:103], a[164:167], v[80:95]\n"
    "ds_read_b64_tr_b16 v[124:125], v50 offset:45312\n"
    "ds_read_b64_tr_b16 v[126:127], v51 offset:45312\n"
    "s_waitcnt lgkmcnt(8)\n"
    "v_mfma_f32_32x32x16_bf16 v[80:95], v[104:107], a[168:171], v[80:95]\n"
    "ds_read_b64_tr_b16 v[96:97], v52 offset:32768\n"
    "ds_read_b64_tr_b16 v[98:99], v53 offset:32768\n"
    "v_mfma_f32_32x32x16_bf16 v[80:95], v[108:111], a[172:175], v[80:95]\n"
    "ds_read_b64_tr_b16 v[100:101], v52 offset:33024\n"
    "ds_read_b64_tr_b16 v[102:103], v53 offset:33024\n"
    "s_waitcnt lgkmcnt(8)\n"
    "v_mfma_f32_32x32x16_bf16 v[80:95], v[112:115], a[176:179], v[80:95]\n"
    "ds_read_b64_tr_b16 v[104:105], v52 offset:36864\n"
    "ds_read_b64_tr_b16 v[106:107], v53 offset:36864\n"
    "v_mfma_f32_32x32x16_bf16 v[80:95], v[116:119], a[180:183], v[80:95]\n"
    "ds_read_b64_tr_b16 v[108:109], v52 offset:37120\n"
    "ds_read_b64_tr_b16 v[110:111], v53 offset:37120\n"
    "s_waitcnt lgkmcnt(8)\n"
    "v_mfma_f32_32x32x16_bf16 v[80:95], v[120:123], a[184:187], v[80:95]\n"
    "ds_read_b64_tr_b16 v[112:113], v52 offset:40960\n"
    "ds_read_b64_tr_b16 v[114:115], v53 offset:40960\n"
    "v_mfma_f32_32x32x16_bf16 v[80:95], v[124:127], a[188:191], v[80:95]\n"
    "ds_read_b64_tr_b16 v[116:117], v52 offset:41216\n"
    "ds_read_b64_tr_b16 v[118:119], v53 offset:41216\n"
    "s_waitcnt lgkmcnt(8)\n"
    "v_mfma_f32_32x32x16_bf16 v[80:95], v[96:99], a[192:195], v[80:95]\n"
    "ds_read_b64_tr_b16 v[120:121], v52 offset:45056\n"
    "ds_read_b64_tr_b16 v[122:123], v53 offset:45056\n"
    "v_mfma_f32_32x32x16_bf16 v[80:95], v[100:103], a[196:199], v[80:95]\n"
    "ds_read_b64_tr_b16 v[124:125], v52 offset:45312\n"
    "ds_read_b64_tr_b16 v[126:127], v53 offset:45312\n"
    "s_waitcnt lgkmcnt(8)\n"
    "v_mfma_f32_32x32x16_bf16 v[80:95], v[104:107], a[200:203], v[80:95]\n"
    "ds_read_b64_tr_b16 v[96:97], v54 offset:32768\n"
    "ds_read_b64_tr_b16 v[98:99], v55 offset:32768\n"
    "v_mfma_f32_32x32x16_bf16 v[80:95], v[108:111], a[204:207], v[80:95]\n"
    "ds_read_b64_tr_b16 v[100:101], v54 offset:33024\n"
    "ds_read_b64_tr_b16 v[102:103], v55 offset:33024\n"
    "s_waitcnt lgkmcnt(8)\n"
    "v_mfma_f32_32x32x16_bf16 v[80:95], v[112:115], a[208:211], v[80:95]\n"
    "ds_read_b64_tr_b16 v[104:105], v54 offset:36864\n"
    "ds_read_b64_tr_b16 v[106:107], v55 offset:36864\n"
    "v_mfma_f32_32x32x16_bf16 v[80:95], v[116:119], a[212:215], v[80:95]\n"
    "ds_read_b64_tr_b16 v[108:109], v54 offset:37120\n"
    "ds_read_b64_tr_b16 v[110:111], v55 offset:37120\n"
    "s_waitcnt lgkmcnt(8)\n"
    "v_mfma_f32_32x32x16_bf16 v[80:95], v[120:123], a[216:219], v[80:95]\n"
    "ds_read_b64_tr_b16 v[112:113], v54 offset:40960\n"
    "ds_read_b64_tr_b16 v[114:115], v55 offset:40960\n"
    "v_mfma_f32_32x32x16_bf16 v[80:95], v[124:127], a[220:223], v[80:95]\n"
    "ds_read_b64_tr_b16 v[116:117], v54 offset:41216\n"
    "ds_read_b64_tr_b16 v[118:119], v55 offset:41216\n"
    "s_waitcnt lgkmcnt(8)\n"
    "v_mfma_f32_32x32x16_bf16 v[80:95], v[96:99], a[224:227], v[80:95]\n"
    "ds_read_b64_tr_b16 v[120:121], v54 offset:45056\n"
    "ds_read_b64_tr_b16 v[122:123], v55 offset:45056\n"
    "v_mfma_f32_32x32x16_bf16 v[80:95], v[100:103], a[228:231], v[80:95]\n"
    "ds_read_b64_tr_b16 v[124:125], v54 offset:45312\n"
    "ds_read_b64_tr_b16 v[126:127], v55 offset:45312\n"
    "s_waitcnt lgkmcnt(8)\n"
    "v_mfma_f32_32x32x16_bf16 v[80:95], v[104:107], a[232:235], v[80:95]\n"
    "v_mfma_f32_32x32x16_bf16 v[80:95], v[108:111], a[236:239], v[80:95]\n"
    "s_waitcnt lgkmcnt(4)\n"
    "v_mfma_f32_32x32x16_bf16 v[80:95], v[112:115], a[240:243], v[80:95]\n"
    "v_mfma_f32_32x32x16_bf16 v[80:95], v[116:119], a[244:247], v[80:95]\n"
    "s_waitcnt lgkmcnt(0)\n"
    "v_mfma_f32_32x32x16_bf16 v[80:95], v[120:123], a[248:251], v[80:95]\n"
    "v_mfma_f32_32x32x16_bf16 v[80:95], v[124:127], a[252:255], v[80:95]\n"
    "s_nop 11\n"
    "buffer_store_dword v80, v136, s[24:27], s19 offen sc1\n"
    "buffer_store_dword v81, v137, s[24:27], s19 offen sc1\n"
    "buffer_store_dword v82, v138, s[24:27], s19 offen sc1\n"
    "buffer_store_dword v83, v139, s[24:27], s19 offen sc1\n"
    "buffer_store_dword v84, v140, s[24:27], s19 offen sc1\n"
    "buffer_store_dword v85, v141, s[24:27], s19 offen sc1\n"
    "buffer_store_dword v86, v142, s[24:27], s19 offen sc1\n"
    "buffer_store_dword v87, v143, s[24:27], s19 offen sc1\n"
    "buffer_store_dword v88, v144, s[24:27], s19 offen sc1\n"
    "buffer_store_dword v89, v145, s[24:27], s19 offen sc1\n"
    "buffer_store_dword v90, v146, s[24:27], s19 offen sc1\n"
    "buffer_store_dword v91, v147, s[24:27], s19 offen sc1\n"
    "buffer_store_dword v92, v148, s[24:27], s19 offen sc1\n"
    "buffer_store_dword v93, v149, s[24:27], s19 offen sc1\n"
    "buffer_store_dword v94, v150, s[24:27], s19 offen sc1\n"
    "buffer_store_dword v95, v151, s[24:27], s19 offen sc1\n"
    "s_endpgm\n"
    "LB_1%=:\n"
    "v_and_b32 v6, 31, v1\n"
    "v_lshrrev_b32 v7, 5, v1\n"
    "v_bfe_u32 v8, v1, 1, 3\n"
    "v_xor_b32 v8, v8, v7\n"
    "v_lshlrev_b32 v8, 4, v8\n"
    "v_lshl_or_b32 v8, v6, 7, v8\n"
    "s_lshl_b32 s6, s66, 13\n"
    "v_add_u32 v18, s6, v8\n"
    "v_xor_b32 v19, 32, v18\n"
    "v_xor_b32 v20, 64, v18\n"
    "v_xor_b32 v21, 96, v18\n"
    "v_add_u32 v38, 0x10000, v18\n"
    "v_add_u32 v39, 0x10000, v19\n"
    "v_add_u32 v40, 0x10000, v20\n"
    "v_add_u32 v41, 0x10000, v21\n"
    "v_bfe_u32 v2, v1, 2, 2\n"
    "v_bfe_u32 v3, v1, 4, 1\n"
    "v_bfe_u32 v4, v1, 1, 1\n"
    "v_lshl_add_u32 v3, v3, 1, v4\n"
    "v_and_b32 v4, 1, v1\n"
    "v_lshl_add_u32 v5, v7, 3, v2\n"
    "v_xor_b32 v9, s67, v2\n"
    "v_lshl_add_u32 v9, v9, 2, v3\n"
    "v_lshlrev_b32 v9, 4, v9\n"
    "v_lshl_add_u32 v9, v4, 3, v9\n"
    "v_lshl_add_u32 v9, v5, 8, v9\n"
    "v_add_u32 v22, 16384, v9\n"
    "v_xor_b32 v23, 128, v22\n"
    "v_add_u32 v24, 0x10000, v22\n"
    "v_add_u32 v25, 0x10000, v23\n"
    "v_lshrrev_b32 v9, 1, v2\n"
    "v_xor_b32 v9, s67, v9\n"
    "v_lshl_add_u32 v9, v9, 2, v3\n"
    "v_lshlrev_b32 v9, 4, v9\n"
    "v_lshl_add_u32 v9, v4, 3, v9\n"
    "v_lshl_add_u32 v42, v5, 7, v9\n"
    "s_mov_b32 s4, 897988541\n"
    "s_mov_b32 s5, 897988541\n"
    "v_mov_b64 v[64:65], s[4:5]\n"
    "v_mov_b64 v[96:97], 0\n"
    "v_mov_b64 v[66:67], s[4:5]\n"
    "v_mov_b64 v[98:99], 0\n"
    "v_mov_b64 v[68:69], s[4:5]\n"
    "v_mov_b64 v[100:101], 0\n"
    "v_mov_b64 v[70:71], s[4:5]\n"
    "v_mov_b64 v[102:103], 0\n"
    "v_mov_b64 v[72:73], s[4:5]\n"
    "v_mov_b64 v[104:105], 0\n"
    "v_mov_b64 v[74:75], s[4:5]\n"
    "v_mov_b64 v[106:107], 0\n"
    "v_mov_b64 v[76:77], s[4:5]\n"
    "v_mov_b64 v[108:109], 0\n"
    "v_mov_b64 v[78:79], s[4:5]\n"
    "v_mov_b64 v[110:111], 0\n"
    "v_mov_b64 v[80:81], s[4:5]\n"
    "v_mov_b64 v[112:113], 0\n"
    "v_mov_b64 v[82:83], s[4:5]\n"
    "v_mov_b64 v[114:115], 0\n"
    "v_mov_b64 v[84:85], s[4:5]\n"
    "v_mov_b64 v[116:117], 0\n"
    "v_mov_b64 v[86:87], s[4:5]\n"
    "v_mov_b64 v[118:119], 0\n"
    "v_mov_b64 v[88:89], s[4:5]\n"
    "v_mov_b64 v[120:121], 0\n"
    "v_mov_b64 v[90:91], s[4:5]\n"
    "v_mov_b64 v[122:123], 0\n"
    "v_mov_b64 v[92:93], s[4:5]\n"
    "v_mov_b64 v[124:125], 0\n"
    "v_mov_b64 v[94:95], s[4:5]\n"
    "v_mov_b64 v[126:127], 0\n"
    "s_waitcnt lgkmcnt(0)\n"
    "buffer_load_dwordx4 v13, s[8:11], s74 offen offset:3072 lds\n"
    "s_add_u32 m0, s77, 49152\n"
    "s_xor_b32 s75, s18, 128\n"
    "s_mulk_i32 s75, 1536\n"
    "buffer_load_dwordx4 v14, s[12:15], s75 offen offset:0 lds\n"
    "buffer_load_dwordx4 v15, s[12:15], s75 offen offset:1024 lds\n"
    "buffer_load_dwordx4 v16, s[12:15], s75 offen offset:2048 lds\n"
    "buffer_load_dwordx4 v17, s[12:15], s75 offen offset:3072 lds\n"
    "s_waitcnt vmcnt(9)\n"
    "s_barrier\n"
    "ds_read_b128 a[0:3], v18 offset:0\n"
    "ds_read_b64_tr_b16 v[128:129], v22 offset:0\n"
    "ds_read_b64_tr_b16 v[130:131], v22 offset:1024\n"
    "ds_read_b64_tr_b16 v[132:133], v23 offset:0\n"
    "ds_read_b64_tr_b16 v[134:135], v23 offset:1024\n"
    "ds_read_b128 a[4:7], v18 offset:4096\n"
    "ds_read_b128 a[8:11], v19 offset:0\n"
    "ds_read_b64_tr_b16 v[136:137], v22 offset:4096\n"
    "ds_read_b64_tr_b16 v[138:139], v22 offset:5120\n"
    "ds_read_b64_tr_b16 v[140:141], v23 offset:4096\n"
    "ds_read_b64_tr_b16 v[142:143], v23 offset:5120\n"
    "ds_read_b128 a[12:15], v19 offset:4096\n"
    "s_add_u32 m0, s77, 98304\n"
    "s_xor_b32 s74, s18, 256\n"
    "buffer_load_dwordx4 v10, s[8:11], s74 offen offset:0 lds\n"
    "buffer_load_dwordx4 v11, s[8:11], s74 offen offset:1024 lds\n"
    "buffer_load_dwordx4 v12, s[8:11], s74 offen offset:2048 lds\n"
    "buffer_load_dwordx4 v13, s[8:11], s74 offen offset:3072 lds\n"
    "s_waitcnt lgkmcnt(6)\n"
    "v_mfma_f32_32x32x16_bf16 v[64:79], a[0:3], v[128:131], v[64:79]\n"
    "s_add_u32 m0, s77, 114688\n"
    "s_xor_b32 s75, s18, 256\n"
    "s_mulk_i32 s75, 1536\n"
    "ds_read_b128 a[16:19], v20 offset:0\n"
    "ds_read_b64_tr_b16 v[144:145], v22 offset:8192\n"
    "buffer_load_dwordx4 v14, s[12:15], s75 offen offset:0 lds\n"
    "v_mfma_f32_32x32x16_bf16 v[96:111], a[0:3], v[132:135], v[96:111]\n"
    "ds_read_b64_tr_b16 v[146:147], v22 offset:9216\n"
    "v_mfma_f32_32x32x16_bf16 v[112:127], a[4:7], v[132:135], v[112:127]\n"
    "ds_read_b64_tr_b16 v[148:149], v23 offset:8192\n"
    "ds_read_b64_tr_b16 v[150:151], v23 offset:9216\n"
    "buffer_load_dwordx4 v15, s[12:15], s75 offen offset:1024 lds\n"
    "v_mfma_f32_32x32x16_bf16 v[80:95], a[4:7], v[128:131], v[80:95]\n"
    "ds_read_b128 a[20:23], v20 offset:4096\n"
    "s_waitcnt lgkmcnt(6)\n"
    "v_mfma_f32_32x32x16_bf16 v[64:79], a[8:11], v[136:139], v[64:79]\n"
    "ds_read_b128 a[24:27], v21 offset:0\n"
    "ds_read_b64_tr_b16 v[152:153], v22 offset:12288\n"
    "buffer_load_dwordx4 v16, s[12:15], s75 offen offset:2048 lds\n"
    "v_mfma_f32_32x32x16_bf16 v[96:111], a[8:11], v[140:143], v[96:111]\n"
    "ds_read_b64_tr_b16 v[154:155], v22 offset:13312\n"
    "v_mfma_f32_32x32x16_bf16 v[112:127], a[12:15], v[140:143], v[112:127]\n"
    "ds_read_b64_tr_b16 v[156:157], v23 offset:12288\n"
    "ds_read_b64_tr_b16 v[158:159], v23 offset:13312\n"
    "buffer_load_dwordx4 v17, s[12:15], s75 offen offset:3072 lds\n"
    "v_mfma_f32_32x32x16_bf16 v[80:95], a[12:15], v[136:139], v[80:95]\n"
    "ds_read_b128 a[28:31], v21 offset:4096\n"
    "s_waitcnt vmcnt(13)\n"
    "v_readfirstlane_b32 s80, v60\n"
    "v_readfirstlane_b32 s81, v61\n"
    "s_add_u32 s80, s80, 1\n"
    "s_add_u32 s81, s81, 1\n"
    "v_lshlrev_b32 v2, 2, v7\n"
    "v_sub_u32 v2, v6, v2\n"
    "v_mov_b32 v9, 0x3f80\n"
    "v_subrev_u32 v3, 0, v2\n"
    "v_lshlrev_b32 v4, 4, v3\n"
    "v_cmp_gt_u32 vcc, 2, v3\n"
    "v_lshlrev_b32 v4, v4, v9\n"
    "s_nop 0\n"
    "v_cndmask_b32 v26, 0, v4, vcc\n"
    "v_subrev_u32 v3, 2, v2\n"
    "v_lshlrev_b32 v4, 4, v3\n"
    "v_cmp_gt_u32 vcc, 2, v3\n"
    "v_lshlrev_b32 v4, v4, v9\n"
    "s_nop 0\n"
    "v_cndmask_b32 v27, 0, v4, vcc\n"
    "v_subrev_u32 v3, 8, v2\n"
    "v_lshlrev_b32 v4, 4, v3\n"
    "v_cmp_gt_u32 vcc, 2, v3\n"
    "v_lshlrev_b32 v4, v4, v9\n"
    "s_nop 0\n"
    "v_cndmask_b32 v28, 0, v4, vcc\n"
    "v_subrev_u32 v3, 10, v2\n"
    "v_lshlrev_b32 v4, 4, v3\n"
    "v_cmp_gt_u32 vcc, 2, v3\n"
    "v_lshlrev_b32 v4, v4, v9\n"
    "s_nop 0\n"
    "v_cndmask_b32 v29, 0, v4, vcc\n"
    "v_subrev_u32 v3, 16, v2\n"
    "v_lshlrev_b32 v4, 4, v3\n"
    "v_cmp_gt_u32 vcc, 2, v3\n"
    "v_lshlrev_b32 v4, v4, v9\n"
    "s_nop 0\n"
    "v_cndmask_b32 v30, 0, v4, vcc\n"
    "v_subrev_u32 v3, 18, v2\n"
    "v_lshlrev_b32 v4, 4, v3\n"
    "v_cmp_gt_u32 vcc, 2, v3\n"
    "v_lshlrev_b32 v4, v4, v9\n"
    "s_nop 0\n"
    "v_cndmask_b32 v31, 0, v4, vcc\n"
    "v_subrev_u32 v3, 24, v2\n"
    "v_lshlrev_b32 v4, 4, v3\n"
    "v_cmp_gt_u32 vcc, 2, v3\n"
    "v_lshlrev_b32 v4, v4, v9\n"
    "s_nop 0\n"
    "v_cndmask_b32 v32, 0, v4, vcc\n"
    "v_subrev_u32 v3, 26, v2\n"
    "v_lshlrev_b32 v4, 4, v3\n"
    "v_cmp_gt_u32 vcc, 2, v3\n"
    "v_lshlrev_b32 v4, v4, v9\n"
    "s_nop 0\n"
    "v_cndmask_b32 v33, 0, v4, vcc\n"
    "v_mov_b32 v34, 0x3f803f80\n"
    "v_mov_b32 v35, 0x3f803f80\n"
    "v_mov_b32 v36, 0x3f803f80\n"
    "v_mov_b32 v37, 0x3f803f80\n"
    "s_cmp_ge_u32 s65, 2\n"
    "s_cbranch_scc1 Lle_11%=\n"
    "s_cmp_eq_u32 s65, 0\n"
    "s_cbranch_scc1 Llx_10%=\n"
    "s_mov_b64 s[4:5], s[38:39]\n"
    "s_sub_u32 s6, s65, 1\n"
    "s_lshl_b32 s6, s6, 10\n"
    "s_lshr_b32 s7, s4, 7\n"
    "s_sub_u32 s7, 3, s7\n"
    "s_and_b32 s7, s7, 7\n"
    "s_lshl_b32 s7, s7, 7\n"
    "v_and_b32 v2, 15, v1\n"
    "s_lshl_b32 s34, s70, 4\n"
    "v_add_u32 v2, s34, v2\n"
    "v_mul_u32_u24 v2, 3072, v2\n"
    "s_add_u32 s7, s7, s6\n"
    "v_add_u32 v2, s7, v2\n"
    "global_load_dword v62, v2, s[4:5]\n"
    "s_branch Lle_11%=\n"
    "Llx_10%=:\n"
    "s_mov_b64 s[4:5], s[56:57]\n"
    "s_mov_b32 s6, 0\n"
    "s_lshr_b32 s7, s4, 7\n"
    "s_sub_u32 s7, 3, s7\n"
    "s_and_b32 s7, s7, 7\n"
    "s_lshl_b32 s7, s7, 7\n"
    "v_and_b32 v2, 15, v1\n"
    "s_lshl_b32 s34, s70, 4\n"
    "v_add_u32 v2, s34, v2\n"
    "v_mul_u32_u24 v2, 1024, v2\n"
    "s_add_u32 s7, s7, s6\n"
    "v_add_u32 v2, s7, v2\n"
    "global_load_dword v62, v2, s[4:5]\n"
    "Lle_11%=:\n"
    "s_waitcnt vmcnt(8)\n"
    "s_barrier\n"
    "s_waitcnt lgkmcnt(6)\n"
    "v_mfma_f32_32x32x16_bf16 v[64:79], a[16:19], v[144:147], v[64:79]\n"
    "s_add_u32 m0, s77, 65536\n"
    "s_xor_b32 s74, s18, 384\n"
    "ds_read_b128 a[32:35], v18 offset:32768\n"
    "ds_read_b64_tr_b16 v[128:129], v22 offset:32768\n"
    "buffer_load_dwordx4 v10, s[8:11], s74 offen offset:0 lds\n"
    "v_mfma_f32_32x32x16_bf16 v[96:111], a[16:19], v[148:151], v[96:111]\n"
    "ds_read_b64_tr_b16 v[130:131], v22 offset:33792\n"
    "v_mfma_f32_32x32x16_bf16 v[112:127], a[20:23], v[148:151], v[112:127]\n"
    "ds_read_b64_tr_b16 v[132:133], v23 offset:32768\n"
    "ds_read_b64_tr_b16 v[134:135], v23 offset:33792\n"
    "buffer_load_dwordx4 v11, s[8:11], s74 offen offset:1024 lds\n"
    "v_mfma_f32_32x32x16_bf16 v[80:95], a[20:23], v[144:147], v[80:95]\n"
    "ds_read_b128 a[36:39], v18 offset:36864\n"
    "s_waitcnt lgkmcnt(6)\n"
    "v_mfma_f32_32x32x16_bf16 v[64:79], a[24:27], v[152:155], v[64:79]\n"
    "ds_read_b128 a[40:43], v19 offset:32768\n"
    "ds_read_b64_tr_b16 v[136:137], v22 offset:36864\n"
    "buffer_load_dwordx4 v12, s[8:11], s74 offen offset:2048 lds\n"
    "v_mfma_f32_32x32x16_bf16 v[96:111], a[24:27], v[156:159], v[96:111]\n"
    "ds_read_b64_tr_b16 v[138:139], v22 offset:37888\n"
    "v_mfma_f32_32x32x16_bf16 v[112:127], a[28:31], v[156:159], v[112:127]\n"
    "ds_read_b64_tr_b16 v[140:141], v23 offset:36864\n"
    "ds_read_b64_tr_b16 v[142:143], v23 offset:37888\n"
    "buffer_load_dwordx4 v13, s[8:11], s74 offen offset:3072 lds\n"
    "v_mfma_f32_32x32x16_bf16 v[80:95], a[28:31], v[152:155], v[80:95]\n"
    "ds_read_b128 a[44:47], v19 offset:36864\n"
    "s_waitcnt lgkmcnt(6)\n"
    "v_mfma_f32_32x32x16_bf16 v[64:79], a[32:35], v[128:131], v[64:79]\n"
    "s_add_u32 m0, s77, 81920\n"
    "s_xor_b32 s75, s18, 384\n"
    "s_mulk_i32 s75, 1536\n"
    "ds_read_b128 a[48:51], v20 offset:32768\n"
    "ds_read_b64_tr_b16 v[144:145], v22 offset:40960\n"
    "buffer_load_dwordx4 v14, s[12:15], s75 offen offset:0 lds\n"
    "v_mfma_f32_32x32x16_bf16 v[96:111], a[32:35], v[132:135], v[96:111]\n"
    "ds_read_b64_tr_b16 v[146:147], v22 offset:41984\n"
    "v_mfma_f32_32x32x16_bf16 v[112:127], a[36:39], v[132:135], v[112:127]\n"
    "ds_read_b64_tr_b16 v[148:149], v23 offset:40960\n"
    "ds_read_b64_tr_b16 v[150:151], v23 offset:41984\n"
    "buffer_load_dwordx4 v15, s[12:15], s75 offen offset:1024 lds\n"
    "v_mfma_f32_32x32x16_bf16 v[80:95], a[36:39], v[128:131], v[80:95]\n"
    "ds_read_b128 a[52:55], v20 offset:36864\n"
    "s_waitcnt lgkmcnt(6)\n"
    "v_mfma_f32_32x32x16_bf16 v[64:79], a[40:43], v[136:139], v[64:79]\n"
    "ds_read_b128 a[56:59], v21 offset:32768\n"
    "ds_read_b64_tr_b16 v[152:153], v22 offset:45056\n"
    "buffer_load_dwordx4 v16, s[12:15], s75 offen offset:2048 lds\n"
    "v_mfma_f32_32x32x16_bf16 v[96:111], a[40:43], v[140:143], v[96:111]\n"
    "ds_read_b64_tr_b16 v[154:155], v22 offset:46080\n"
    "v_mfma_f32_32x32x16_bf16 v[112:127], a[44:47], v[140:143], v[112:127]\n"
    "ds_read_b64_tr_b16 v[156:157], v23 offset:45056\n"
    "ds_read_b64_tr_b16 v[158:159], v23 offset:46080\n"
    "buffer_load_dwordx4 v17, s[12:15], s75 offen offset:3072 lds\n"
    "v_mfma_f32_32x32x16_bf16 v[80:95], a[44:47], v[136:139], v[80:95]\n"
    "ds_read_b128 a[60:63], v21 offset:36864\n"
    "s_mov_b32 s4, 897988541\n"
    "s_mov_b32 s5, 897988541\n"
    "v_mov_b64 v[192:193], s[4:5]\n"
    "v_mov_b64 v[194:195], s[4:5]\n"
    "v_mov_b64 v[196:197], s[4:5]\n"
    "v_mov_b64 v[198:199], s[4:5]\n"
    "v_mov_b64 v[200:201], s[4:5]\n"
    "v_mov_b64 v[202:203], s[4:5]\n"
    "v_mov_b64 v[204:205], s[4:5]\n"
    "v_mov_b64 v[206:207], s[4:5]\n"
    "v_mov_b64 v[208:209], s[4:5]\n"
    "v_mov_b64 v[210:211], s[4:5]\n"
    "v_mov_b64 v[212:213], s[4:5]\n"
    "v_mov_b64 v[214:215], s[4:5]\n"
    "v_mov_b64 v[216:217], s[4:5]\n"
    "v_mov_b64 v[218:219], s[4:5]\n"
    "v_mov_b64 v[220:221], s[4:5]\n"
    "v_mov_b64 v[222:223], s[4:5]\n"
    "s_mov_b32 s82, 1069066811\n"
    "s_mov_b32 s83, 3049366176\n"
    "s_mov_b32 s84, 897988541\n"
    "s_lshl_b32 s4, s67, 12\n"
    "s_add_u32 s4, s4, 131072\n"
    "v_lshl_add_u32 v43, v1, 4, s4\n"
    "s_lshl_b32 s4, s78, 4\n"
    "s_add_u32 s5, s4, s68\n"
    "s_lshl_b32 s6, s5, 10\n"
    "s_add_u32 s6, s6, 262144\n"
    "v_lshl_add_u32 v44, v1, 4, s6\n"
    "v_bfe_u32 v4, v1, 4, 1\n"
    "v_lshrrev_b32 v5, 5, v1\n"
    "v_lshl_add_u32 v5, v4, 1, v5\n"
    "v_lshlrev_b32 v46, 4, v5\n"
    "v_cmp_ne_u32 s[86:87], 0, v4\n"
    "s_and_b32 s6, s5, -8\n"
    "s_lshl_b32 s6, s6, 6\n"
    "s_and_b32 s7, s68, 7\n"
    "s_lshl_b32 s7, s7, 4\n"
    "s_add_u32 s6, s6, s7\n"
    "s_add_u32 s6, s6, 32768\n"
    "v_lshlrev_b32 v46, 3, v46\n"
    "v_add_u32 v45, s6, v46\n"
    "v_and_b32 v2, 7, v1\n"
    "v_lshl_add_u32 v46, v2, 4, v46\n"
    "s_lshl_b32 s6, s5, 4\n"
    "s_add_u32 s6, s6, 524288\n"
    "s_cmp_eq_u32 s66, 0\n"
    "s_cselect_b32 s16, s6, 2146435072\n"
    "s_lshl_b32 s6, s78, 1\n"
    "s_add_u32 s6, s6, s66\n"
    "s_lshl_b32 s6, s6, 7\n"
    "s_add_u32 s6, s6, 524288\n"
    "v_and_b32 v2, 7, v1\n"
    "v_lshl_add_u32 v47, v2, 4, s6\n"
    "s_waitcnt vmcnt(8)\n"
    "s_barrier\n"
    "s_waitcnt lgkmcnt(6)\n"
    "v_mfma_f32_32x32x16_bf16 v[64:79], a[48:51], v[144:147], v[64:79]\n"
    "s_add_u32 m0, s77, 0\n"
    "s_xor_b32 s74, s18, 512\n"
    "ds_read_b128 a[64:67], v38 offset:32768\n"
    "ds_read_b64_tr_b16 v[128:129], v24 offset:32768\n"
    "buffer_load_dwordx4 v10, s[8:11], s74 offen offset:0 lds\n"
    "v_mfma_f32_32x32x16_bf16 v[96:111], a[48:51], v[148:151], v[96:111]\n"
    "ds_read_b64_tr_b16 v[130:131], v24 offset:33792\n"
    "v_mfma_f32_32x32x16_bf16 v[112:127], a[52:55], v[148:151], v[112:127]\n"
    "ds_read_b64_tr_b16 v[132:133], v25 offset:32768\n"
    "ds_read_b64_tr_b16 v[134:135], v25 offset:33792\n"
    "buffer_load_dwordx4 v11, s[8:11], s74 offen offset:1024 lds\n"
    "v_mfma_f32_32x32x16_bf16 v[80:95], a[52:55], v[144:147], v[80:95]\n"
    "ds_read_b128 a[68:71], v38 offset:36864\n"
    "s_waitcnt lgkmcnt(6)\n"
    "v_mfma_f32_32x32x16_bf16 v[64:79], a[56:59], v[152:155], v[64:79]\n"
    "ds_read_b128 a[72:75], v39 offset:32768\n"
    "ds_read_b64_tr_b16 v[136:137], v24 offset:36864\n"
    "buffer_load_dwordx4 v12, s[8:11], s74 offen offset:2048 lds\n"
    "v_mfma_f32_32x32x16_bf16 v[96:111], a[56:59], v[156:159], v[96:111]\n"
    "ds_read_b64_tr_b16 v[138:139], v24 offset:37888\n"
    "v_mfma_f32_32x32x16_bf16 v[112:127], a[60:63], v[156:159], v[112:127]\n"
    "ds_read_b64_tr_b16 v[140:141], v25 offset:36864\n"
    "ds_read_b64_tr_b16 v[142:143], v25 offset:37888\n"
    "buffer_load_dwordx4 v13, s[8:11], s74 offen offset:3072 lds\n"
    "v_mfma_f32_32x32x16_bf16 v[80:95], a[60:63], v[152:155], v[80:95]\n"
    "ds_read_b128 a[76:79], v39 offset:36864\n"
    "s_waitcnt lgkmcnt(6)\n"
    "v_mfma_f32_32x32x16_bf16 v[64:79], a[64:67], v[128:131], v[64:79]\n"
    "s_add_u32 m0, s77, 16384\n"
    "s_xor_b32 s75, s18, 512\n"
    "s_mulk_i32 s75, 1536\n"
    "ds_read_b128 a[80:83], v40 offset:32768\n"
    "ds_read_b64_tr_b16 v[144:145], v24 offset:40960\n"
    "buffer_load_dwordx4 v14, s[12:15], s75 offen offset:0 lds\n"
    "v_mfma_f32_32x32x16_bf16 v[96:111], a[64:67], v[132:135], v[96:111]\n"
    "ds_read_b64_tr_b16 v[146:147], v24 offset:41984\n"
    "s_lshl_b32 s4, s68, 17\n"
    "s_lshl_b32 s5, s66, 15\n"
    "v_mfma_f32_32x32x16_bf16 v[112:127], a[68:71], v[132:135], v[112:127]\n"
    "ds_read_b64_tr_b16 v[148:149], v25 offset:40960\n"
    "ds_read_b64_tr_b16 v[150:151], v25 offset:41984\n"
    "buffer_load_dwordx4 v15, s[12:15], s75 offen offset:1024 lds\n"
    "s_add_u32 s4, s4, s5\n"
    "v_mfma_f32_32x32x16_bf16 v[80:95], a[68:71], v[128:131], v[80:95]\n"
    "ds_read_b128 a[84:87], v40 offset:36864\n"
    "s_lshl_b32 s6, s78, 11\n"
    "s_add_u32 s4, s4, s6\n"
    "s_waitcnt lgkmcnt(6)\n"
    "v_mfma_f32_32x32x16_bf16 v[64:79], a[72:75], v[136:139], v[64:79]\n"
    "ds_read_b128 a[88:91], v41 offset:32768\n"
    "ds_read_b64_tr_b16 v[152:153], v24 offset:45056\n"
    "buffer_load_dwordx4 v16, s[12:15], s75 offen offset:2048 lds\n"
    "v_mfma_f32_32x32x16_bf16 v[96:111], a[72:75], v[140:143], v[96:111]\n"
    "ds_read_b64_tr_b16 v[154:155], v24 offset:46080\n"
    "s_add_u32 s4, s4, 1048576\n"
    "v_lshl_add_u32 v49, v1, 4, s4\n"
    "v_mfma_f32_32x32x16_bf16 v[112:127], a[76:79], v[140:143], v[112:127]\n"
    "ds_read_b64_tr_b16 v[156:157], v25 offset:45056\n"
    "ds_read_b64_tr_b16 v[158:159], v25 offset:46080\n"
    "buffer_load_dwordx4 v17, s[12:15], s75 offen offset:3072 lds\n"
    "s_lshl_b32 s4, s68, 3\n"
    "v_mfma_f32_32x32x16_bf16 v[80:95], a[76:79], v[136:139], v[80:95]\n"
    "ds_read_b128 a[92:95], v41 offset:36864\n"
    "s_add_u32 s4, s4, s69\n"
    "s_lshl_b32 s4, s4, 7\n"
    "s_waitcnt vmcnt(8)\n"
    "s_barrier\n"
    "s_waitcnt lgkmcnt(6)\n"
    "v_mfma_f32_32x32x16_bf16 v[64:79], a[80:83], v[144:147], v[64:79]\n"
    "s_add_u32 m0, s77, 32768\n"
    "s_xor_b32 s74, s18, 640\n"
    "ds_read_b128 a[96:99], v38 offset:0\n"
    "ds_read_b64_tr_b16 v[128:129], v24 offset:0\n"
    "buffer_load_dwordx4 v10, s[8:11], s74 offen offset:0 lds\n"
    "v_mfma_f32_32x32x16_bf16 v[96:111], a[80:83], v[148:151], v[96:111]\n"
    "ds_read_b64_tr_b16 v[130:131], v24 offset:1024\n"
    "s_add_u32 s4, s4, 589824\n"
    "s_cmp_eq_u32 s65, 0\n"
    "s_cselect_b32 s17, s4, 2146435072\n"
    "v_mfma_f32_32x32x16_bf16 v[112:127], a[84:87], v[148:151], v[112:127]\n"
    "ds_read_b64_tr_b16 v[132:133], v25 offset:0\n"
    "ds_read_b64_tr_b16 v[134:135], v25 offset:1024\n"
    "buffer_load_dwordx4 v11, s[8:11], s74 offen offset:1024 lds\n"
    "s_and_b32 s5, s69, 6\n"
    "v_mfma_f32_32x32x16_bf16 v[80:95], a[84:87], v[144:147], v[80:95]\n"
    "ds_read_b128 a[100:103], v38 offset:4096\n"
    "s_lshl_b32 s5, s5, 7\n"
    "s_sub_u32 s7, 524288, s5\n"
    "s_waitcnt lgkmcnt(6)\n"
    "v_mfma_f32_32x32x16_bf16 v[64:79], a[88:91], v[152:155], v[64:79]\n"
    "ds_read_b128 a[104:107], v39 offset:0\n"
    "ds_read_b64_tr_b16 v[136:137], v24 offset:4096\n"
    "buffer_load_dwordx4 v12, s[8:11], s74 offen offset:2048 lds\n"
    "s_add_u32 s28, s56, s5\n"
    "s_addc_u32 s29, s57, 0\n"
    "v_mfma_f32_32x32x16_bf16 v[96:111], a[88:91], v[156:159], v[96:111]\n"
    "ds_read_b64_tr_b16 v[138:139], v24 offset:5120\n"
    "s_and_b32 s29, s29, 0xffff\n"
    "s_mov_b32 s30, s7\n"
    "v_mfma_f32_32x32x16_bf16 v[112:127], a[92:95], v[156:159], v[112:127]\n"
    "ds_read_b64_tr_b16 v[140:141], v25 offset:4096\n"
    "ds_read_b64_tr_b16 v[142:143], v25 offset:5120\n"
    "buffer_load_dwordx4 v13, s[8:11], s74 offen offset:3072 lds\n"
    "s_mov_b32 s31, 0x20000\n"
    "v_mfma_f32_32x32x16_bf16 v[80:95], a[92:95], v[152:155], v[80:95]\n"
    "ds_read_b128 a[108:111], v39 offset:4096\n"
    "v_mov_b32 v50, s80\n"
    "v_mov_b32 v51, s81\n"
    "s_waitcnt lgkmcnt(6)\n"
    "v_mfma_f32_32x32x16_bf16 v[64:79], a[96:99], v[128:131], v[64:79]\n"
    "s_add_u32 m0, s77, 49152\n"
    "s_xor_b32 s75, s18, 640\n"
    "s_mulk_i32 s75, 1536\n"
    "ds_read_b128 a[112:115], v40 offset:0\n"
    "ds_read_b64_tr_b16 v[144:145], v24 offset:8192\n"
    "buffer_load_dwordx4 v14, s[12:15], s75 offen offset:0 lds\n"
    "v_mfma_f32_32x32x16_bf16 v[96:111], a[96:99], v[132:135], v[96:111]\n"
    "ds_read_b64_tr_b16 v[146:147], v24 offset:9216\n"
    "v_mfma_f32_32x32x16_bf16 v[112:127], a[100:103], v[132:135], v[112:127]\n"
    "ds_read_b64_tr_b16 v[148:149], v25 offset:8192\n"
    "ds_read_b64_tr_b16 v[150:151], v25 offset:9216\n"
    "buffer_load_dwordx4 v15, s[12:15], s75 offen offset:1024 lds\n"
    "v_mfma_f32_32x32x16_bf16 v[80:95], a[100:103], v[128:131], v[80:95]\n"
    "ds_read_b128 a[116:119], v40 offset:4096\n"
    "s_waitcnt lgkmcnt(6)\n"
    "v_mfma_f32_32x32x16_bf16 v[64:79], a[104:107], v[136:139], v[64:79]\n"
    "ds_read_b128 a[120:123], v41 offset:0\n"
    "ds_read_b64_tr_b16 v[152:153], v24 offset:12288\n"
    "buffer_load_dwordx4 v16, s[12:15], s75 offen offset:2048 lds\n"
    "v_mfma_f32_32x32x16_bf16 v[96:111], a[104:107], v[140:143], v[96:111]\n"
    "ds_read_b64_tr_b16 v[154:155], v24 offset:13312\n"
    "v_mfma_f32_32x32x16_bf16 v[112:127], a[108:111], v[140:143], v[112:127]\n"
    "ds_read_b64_tr_b16 v[156:157], v25 offset:12288\n"
    "ds_read_b64_tr_b16 v[158:159], v25 offset:13312\n"
    "buffer_load_dwordx4 v17, s[12:15], s75 offen offset:3072 lds\n"
    "v_mfma_f32_32x32x16_bf16 v[80:95], a[108:111], v[136:139], v[80:95]\n"
    "ds_read_b128 a[124:127], v41 offset:4096\n"
    "s_waitcnt vmcnt(8)\n"
    "s_barrier\n"
    "s_waitcnt lgkmcnt(6)\n"
    "v_mfma_f32_32x32x16_bf16 v[64:79], a[112:115], v[144:147], v[64:79]\n"
    "s_add_u32 m0, s77, 98304\n"
    "s_xor_b32 s74, s18, 768\n"
    "ds_read_b128 a[128:131], v18 offset:0\n"
    "ds_read_b64_tr_b16 v[128:129], v22 offset:0\n"
    "buffer_load_dwordx4 v10, s[8:11], s74 offen offset:0 lds\n"
    "v_mfma_f32_32x32x16_bf16 v[96:111], a[112:115], v[148:151], v[96:111]\n"
    "ds_read_b64_tr_b16 v[130:131], v22 offset:1024\n"
    "v_mfma_f32_32x32x16_bf16 v[112:127], a[116:119], v[148:151], v[112:127]\n"
    "ds_read_b64_tr_b16 v[132:133], v23 offset:0\n"
    "ds_read_b64_tr_b16 v[134:135], v23 offset:1024\n"
    "buffer_load_dwordx4 v11, s[8:11], s74 offen offset:1024 lds\n"
    "v_mfma_f32_32x32x16_bf16 v[80:95], a[116:119], v[144:147], v[80:95]\n"
    "ds_read_b128 a[132:135], v18 offset:4096\n"
    "s_waitcnt lgkmcnt(6)\n"
    "v_mfma_f32_32x32x16_bf16 v[64:79], a[120:123], v[152:155], v[64:79]\n"
    "ds_read_b128 a[136:139], v19 offset:0\n"
    "ds_read_b64_tr_b16 v[136:137], v22 offset:4096\n"
    "buffer_load_dwordx4 v12, s[8:11], s74 offen offset:2048 lds\n"
    "v_mfma_f32_32x32x16_bf16 v[96:111], a[120:123], v[156:159], v[96:111]\n"
    "ds_read_b64_tr_b16 v[138:139], v22 offset:5120\n"
    "v_mfma_f32_32x32x16_bf16 v[112:127], a[124:127], v[156:159], v[112:127]\n"
    "ds_read_b64_tr_b16 v[140:141], v23 offset:4096\n"
    "ds_read_b64_tr_b16 v[142:143], v23 offset:5120\n"
    "buffer_load_dwordx4 v13, s[8:11], s74 offen offset:3072 lds\n"
    "v_mfma_f32_32x32x16_bf16 v[80:95], a[124:127], v[152:155], v[80:95]\n"
    "ds_read_b128 a[140:143], v19 offset:4096\n"
    "s_waitcnt lgkmcnt(6)\n"
    "v_mfma_f32_32x32x16_bf16 v[64:79], a[128:131], v[128:131], v[64:79]\n"
    "s_add_u32 m0, s77, 114688\n"
    "s_xor_b32 s75, s18, 768\n"
    "s_mulk_i32 s75, 1536\n"
    "ds_read_b128 a[144:147], v20 offset:0\n"
    "ds_read_b64_tr_b16 v[144:145], v22 offset:8192\n"
    "buffer_load_dwordx4 v14, s[12:15], s75 offen offset:0 lds\n"
    "v_mfma_f32_32x32x16_bf16 v[96:111], a[128:131], v[132:135], v[96:111]\n"
    "ds_read_b64_tr_b16 v[146:147], v22 offset:9216\n"
    "v_mfma_f32_32x32x16_bf16 v[112:127], a[132:135], v[132:135], v[112:127]\n"
    "ds_read_b64_tr_b16 v[148:149], v23 offset:8192\n"
    "ds_read_b64_tr_b16 v[150:151], v23 offset:9216\n"
    "buffer_load_dwordx4 v15, s[12:15], s75 offen offset:1024 lds\n"
    "v_mfma_f32_32x32x16_bf16 v[80:95], a[132:135], v[128:131], v[80:95]\n"
    "ds_read_b128 a[148:151], v20 offset:4096\n"
    "s_waitcnt lgkmcnt(6)\n"
    "v_mfma_f32_32x32x16_bf16 v[64:79], a[136:139], v[136:139], v[64:79]\n"
    "ds_read_b128 a[152:155], v21 offset:0\n"
    "ds_read_b64_tr_b16 v[152:153], v22 offset:12288\n"
    "buffer_load_dwordx4 v16, s[12:15], s75 offen offset:2048 lds\n"
    "v_mfma_f32_32x32x16_bf16 v[96:111], a[136:139], v[140:143], v[96:111]\n"
    "ds_read_b64_tr_b16 v[154:155], v22 offset:13312\n"
    "v_mfma_f32_32x32x16_bf16 v[112:127], a[140:143], v[140:143], v[112:127]\n"
    "ds_read_b64_tr_b16 v[156:157], v23 offset:12288\n"
    "ds_read_b64_tr_b16 v[158:159], v23 offset:13312\n"
    "buffer_load_dwordx4 v17, s[12:15], s75 offen offset:3072 lds\n"
    "v_mfma_f32_32x32x16_bf16 v[80:95], a[140:143], v[136:139], v[80:95]\n"
    "ds_read_b128 a[156:159], v21 offset:4096\n"
    "s_waitcnt vmcnt(8)\n"
    "s_barrier\n"
    "s_waitcnt lgkmcnt(6)\n"
    "v_mfma_f32_32x32x16_bf16 v[64:79], a[144:147], v[144:147], v[64:79]\n"
    "s_add_u32 m0, s77, 65536\n"
    "s_xor_b32 s74, s18, 896\n"
    "ds_read_b128 a[160:163], v18 offset:32768\n"
    "ds_read_b64_tr_b16 v[128:129], v22 offset:32768\n"
    "buffer_load_dwordx4 v10, s[8:11], s74 offen offset:0 lds\n"
    "v_mfma_f32_32x32x16_bf16 v[96:111], a[144:147], v[148:151], v[96:111]\n"
    "ds_read_b64_tr_b16 v[130:131], v22 offset:33792\n"
    "v_mfma_f32_32x32x16_bf16 v[112:127], a[148:151], v[148:151], v[112:127]\n"
    "ds_read_b64_tr_b16 v[132:133], v23 offset:32768\n"
    "ds_read_b64_tr_b16 v[134:135], v23 offset:33792\n"
    "buffer_load_dwordx4 v11, s[8:11], s74 offen offset:1024 lds\n"
    "v_mfma_f32_32x32x16_bf16 v[80:95], a[148:151], v[144:147], v[80:95]\n"
    "ds_read_b128 a[164:167], v18 offset:36864\n"
    "s_waitcnt lgkmcnt(6)\n"
    "v_mfma_f32_32x32x16_bf16 v[64:79], a[152:155], v[152:155], v[64:79]\n"
    "ds_read_b128 a[168:171], v19 offset:32768\n"
    "ds_read_b64_tr_b16 v[136:137], v22 offset:36864\n"
    "buffer_load_dwordx4 v12, s[8:11], s74 offen offset:2048 lds\n"
    "v_mfma_f32_32x32x16_bf16 v[96:111], a[152:155], v[156:159], v[96:111]\n"
    "ds_read_b64_tr_b16 v[138:139], v22 offset:37888\n"
    "v_mfma_f32_32x32x16_bf16 v[112:127], a[156:159], v[156:159], v[112:127]\n"
    "ds_read_b64_tr_b16 v[140:141], v23 offset:36864\n"
    "ds_read_b64_tr_b16 v[142:143], v23 offset:37888\n"
    "buffer_load_dwordx4 v13, s[8:11], s74 offen offset:3072 lds\n"
    "v_mfma_f32_32x32x16_bf16 v[80:95], a[156:159], v[152:155], v[80:95]\n"
    "ds_read_b128 a[172:175], v19 offset:36864\n"
    "s_waitcnt lgkmcnt(6)\n"
    "v_mfma_f32_32x32x16_bf16 v[64:79], a[160:163], v[128:131], v[64:79]\n"
    "s_add_u32 m0, s77, 81920\n"
    "s_xor_b32 s75, s18, 896\n"
    "s_mulk_i32 s75, 1536\n"
    "ds_read_b128 a[176:179], v20 offset:32768\n"
    "ds_read_b64_tr_b16 v[144:145], v22 offset:40960\n"
    "buffer_load_dwordx4 v14, s[12:15], s75 offen offset:0 lds\n"
    "v_mfma_f32_32x32x16_bf16 v[96:111], a[160:163], v[132:135], v[96:111]\n"
    "ds_read_b64_tr_b16 v[146:147], v22 offset:41984\n"
    "v_mfma_f32_32x32x16_bf16 v[112:127], a[164:167], v[132:135], v[112:127]\n"
    "ds_read_b64_tr_b16 v[148:149], v23 offset:40960\n"
    "ds_read_b64_tr_b16 v[150:151], v23 offset:41984\n"
    "buffer_load_dwordx4 v15, s[12:15], s75 offen offset:1024 lds\n"
    "v_mfma_f32_32x32x16_bf16 v[80:95], a[164:167], v[128:131], v[80:95]\n"
    "ds_read_b128 a[180:183], v20 offset:36864\n"
    "s_waitcnt lgkmcnt(6)\n"
    "v_mfma_f32_32x32x16_bf16 v[64:79], a[168:171], v[136:139], v[64:79]\n"
    "ds_read_b128 a[184:187], v21 offset:32768\n"
    "ds_read_b64_tr_b16 v[152:153], v22 offset:45056\n"
    "buffer_load_dwordx4 v16, s[12:15], s75 offen offset:2048 lds\n"
    "v_mfma_f32_32x32x16_bf16 v[96:111], a[168:171], v[140:143], v[96:111]\n"
    "ds_read_b64_tr_b16 v[154:155], v22 offset:46080\n"
    "v_mfma_f32_32x32x16_bf16 v[112:127], a[172:175], v[140:143], v[112:127]\n"
    "ds_read_b64_tr_b16 v[156:157], v23 offset:45056\n"
    "ds_read_b64_tr_b16 v[158:159], v23 offset:46080\n"
    "buffer_load_dwordx4 v17, s[12:15], s75 offen offset:3072 lds\n"
    "v_mfma_f32_32x32x16_bf16 v[80:95], a[172:175], v[136:139], v[80:95]\n"
    "ds_read_b128 a[188:191], v21 offset:36864\n"
    "v_lshrrev_b32 v2, 3, v1\n"
    "s_lshl_b32 s4, s65, 5\n"
    "v_add_u32 v2, s4, v2\n"
    "v_bfe_u32 v3, v1, 4, 1\n"
    "v_lshlrev_b32 v3, 2, v3\n"
    "v_xor_b32 v3, v3, v1\n"
    "v_and_b32 v3, 7, v3\n"
    "v_lshlrev_b32 v3, 4, v3\n"
    "v_mul_u32_u24 v4, 3072, v2\n"
    "v_add_u32 v10, v4, v3\n"
    "v_lshrrev_b32 v2, 4, v1\n"
    "v_lshlrev_b32 v3, 2, v2\n"
    "v_xor_b32 v3, v3, v1\n"
    "v_and_b32 v3, 15, v3\n"
    "v_lshlrev_b32 v3, 4, v3\n"
    "v_lshl_or_b32 v3, v2, 10, v3\n"
    "s_lshl_b32 s4, s65, 14\n"
    "v_add_u32 v14, s4, v3\n"
    "v_add_u32 v11, 23552, v10\n"
    "v_add_u32 v15, 3072, v14\n"
    "v_add_u32 v12, 47104, v10\n"
    "v_add_u32 v16, 6144, v14\n"
    "v_add_u32 v13, 70656, v10\n"
    "v_add_u32 v17, 9216, v14\n"
    "s_waitcnt vmcnt(8)\n"
    "s_barrier\n"
    "s_waitcnt lgkmcnt(6)\n"
    "v_mfma_f32_32x32x16_bf16 v[64:79], a[176:179], v[144:147], v[64:79]\n"
    "s_add_u32 m0, s77, 0\n"
    "s_xor_b32 s75, s18, 0\n"
    "s_mulk_i32 s75, 1536\n"
    "ds_read_b128 a[192:195], v38 offset:32768\n"
    "ds_read_b64_tr_b16 v[128:129], v24 offset:32768\n"
    "buffer_load_dwordx4 v10, s[12:15], s75 offen offset:0 lds\n"
    "v_mfma_f32_32x32x16_bf16 v[96:111], a[176:179], v[148:151], v[96:111]\n"
    "ds_read_b64_tr_b16 v[130:131], v24 offset:33792\n"
    "v_mfma_f32_32x32x16_bf16 v[112:127], a[180:183], v[148:151], v[112:127]\n"
    "ds_read_b64_tr_b16 v[132:133], v25 offset:32768\n"
    "ds_read_b64_tr_b16 v[134:135], v25 offset:33792\n"
    "buffer_load_dwordx4 v11, s[12:15], s75 offen offset:1024 lds\n"
    "v_mfma_f32_32x32x16_bf16 v[80:95], a[180:183], v[144:147], v[80:95]\n"
    "ds_read_b128 a[196:199], v38 offset:36864\n"
    "s_waitcnt lgkmcnt(6)\n"
    "v_mfma_f32_32x32x16_bf16 v[64:79], a[184:187], v[152:155], v[64:79]\n"
    "ds_read_b128 a[200:203], v39 offset:32768\n"
    "ds_read_b64_tr_b16 v[136:137], v24 offset:36864\n"
    "buffer_load_dwordx4 v12, s[12:15], s75 offen offset:2048 lds\n"
    "v_mfma_f32_32x32x16_bf16 v[96:111], a[184:187], v[156:159], v[96:111]\n"
    "ds_read_b64_tr_b16 v[138:139], v24 offset:37888\n"
    "v_mfma_f32_32x32x16_bf16 v[112:127], a[188:191], v[156:159], v[112:127]\n"
    "ds_read_b64_tr_b16 v[140:141], v25 offset:36864\n"
    "ds_read_b64_tr_b16 v[142:143], v25 offset:37888\n"
    "buffer_load_dwordx4 v13, s[12:15], s75 offen offset:3072 lds\n"
    "v_mfma_f32_32x32x16_bf16 v[80:95], a[188:191], v[152:155], v[80:95]\n"
    "ds_read_b128 a[204:207], v39 offset:36864\n"
    "s_waitcnt lgkmcnt(6)\n"
    "v_mfma_f32_32x32x16_bf16 v[64:79], a[192:195], v[128:131], v[64:79]\n"
    "s_add_u32 m0, s77, 16384\n"
    "s_xor_b32 s75, s18, 256\n"
    "s_mulk_i32 s75, 1536\n"
    "ds_read_b128 a[208:211], v40 offset:32768\n"
    "ds_read_b64_tr_b16 v[144:145], v24 offset:40960\n"
    "buffer_load_dwordx4 v10, s[12:15], s75 offen offset:0 lds\n"
    "v_mfma_f32_32x32x16_bf16 v[96:111], a[192:195], v[132:135], v[96:111]\n"
    "ds_read_b64_tr_b16 v[146:147], v24 offset:41984\n"
    "v_mfma_f32_32x32x16_bf16 v[112:127], a[196:199], v[132:135], v[112:127]\n"
    "ds_read_b64_tr_b16 v[148:149], v25 offset:40960\n"
    "ds_read_b64_tr_b16 v[150:151], v25 offset:41984\n"
    "buffer_load_dwordx4 v11, s[12:15], s75 offen offset:1024 lds\n"
    "v_mfma_f32_32x32x16_bf16 v[80:95], a[196:199], v[128:131], v[80:95]\n"
    "ds_read_b128 a[212:215], v40 offset:36864\n"
    "s_waitcnt lgkmcnt(6)\n"
    "v_mfma_f32_32x32x16_bf16 v[64:79], a[200:203], v[136:139], v[64:79]\n"
    "ds_read_b128 a[216:219], v41 offset:32768\n"
    "ds_read_b64_tr_b16 v[152:153], v24 offset:45056\n"
    "buffer_load_dwordx4 v12, s[12:15], s75 offen offset:2048 lds\n"
    "v_mfma_f32_32x32x16_bf16 v[96:111], a[200:203], v[140:143], v[96:111]\n"
    "ds_read_b64_tr_b16 v[154:155], v24 offset:46080\n"
    "v_mfma_f32_32x32x16_bf16 v[112:127], a[204:207], v[140:143], v[112:127]\n"
    "ds_read_b64_tr_b16 v[156:157], v25 offset:45056\n"
    "ds_read_b64_tr_b16 v[158:159], v25 offset:46080\n"
    "buffer_load_dwordx4 v13, s[12:15], s75 offen offset:3072 lds\n"
    "v_mfma_f32_32x32x16_bf16 v[80:95], a[204:207], v[136:139], v[80:95]\n"
    "ds_read_b128 a[220:223], v41 offset:36864\n"
    "s_waitcnt vmcnt(8)\n"
    "s_barrier\n"
    "s_waitcnt lgkmcnt(6)\n"
    "v_mfma_f32_32x32x16_bf16 v[64:79], a[208:211], v[144:147], v[64:79]\n"
    "s_add_u32 m0, s77, 32768\n"
    "s_xor_b32 s75, s18, 512\n"
    "s_mulk_i32 s75, 1536\n"
    "ds_read_b128 a[224:227], v38 offset:0\n"
    "ds_read_b64_tr_b16 v[128:129], v24 offset:0\n"
    "buffer_load_dwordx4 v10, s[12:15], s75 offen offset:0 lds\n"
    "v_mfma_f32_32x32x16_bf16 v[96:111], a[208:211], v[148:151], v[96:111]\n"
    "ds_read_b64_tr_b16 v[130:131], v24 offset:1024\n"
    "v_mfma_f32_32x32x16_bf16 v[112:127], a[212:215], v[148:151], v[112:127]\n"
    "ds_read_b64_tr_b16 v[132:133], v25 offset:0\n"
    "ds_read_b64_tr_b16 v[134:135], v25 offset:1024\n"
    "buffer_load_dwordx4 v11, s[12:15], s75 offen offset:1024 lds\n"
    "v_mfma_f32_32x32x16_bf16 v[80:95], a[212:215], v[144:147], v[80:95]\n"
    "ds_read_b128 a[228:231], v38 offset:4096\n"
    "s_waitcnt lgkmcnt(6)\n"
    "v_mfma_f32_32x32x16_bf16 v[64:79], a[216:219], v[152:155], v[64:79]\n"
    "ds_read_b128 a[232:235], v39 offset:0\n"
    "ds_read_b64_tr_b16 v[136:137], v24 offset:4096\n"
    "buffer_load_dwordx4 v12, s[12:15], s75 offen offset:2048 lds\n"
    "v_mfma_f32_32x32x16_bf16 v[96:111], a[216:219], v[156:159], v[96:111]\n"
    "ds_read_b64_tr_b16 v[138:139], v24 offset:5120\n"
    "v_mfma_f32_32x32x16_bf16 v[112:127], a[220:223], v[156:159], v[112:127]\n"
    "ds_read_b64_tr_b16 v[140:141], v25 offset:4096\n"
    "ds_read_b64_tr_b16 v[142:143], v25 offset:5120\n"
    "buffer_load_dwordx4 v13, s[12:15], s75 offen offset:3072 lds\n"
    "v_mfma_f32_32x32x16_bf16 v[80:95], a[220:223], v[152:155], v[80:95]\n"
    "ds_read_b128 a[236:239], v39 offset:4096\n"
    "s_waitcnt lgkmcnt(6)\n"
    "v_mfma_f32_32x32x16_bf16 v[64:79], a[224:227], v[128:131], v[64:79]\n"
    "s_add_u32 m0, s77, 49152\n"
    "s_xor_b32 s75, s18, 768\n"
    "s_mulk_i32 s75, 1536\n"
    "ds_read_b128 a[240:243], v40 offset:0\n"
    "ds_read_b64_tr_b16 v[144:145], v24 offset:8192\n"
    "buffer_load_dwordx4 v10, s[12:15], s75 offen offset:0 lds\n"
    "v_mfma_f32_32x32x16_bf16 v[96:111], a[224:227], v[132:135], v[96:111]\n"
    "ds_read_b64_tr_b16 v[146:147], v24 offset:9216\n"
    "v_mfma_f32_32x32x16_bf16 v[112:127], a[228:231], v[132:135], v[112:127]\n"
    "ds_read_b64_tr_b16 v[148:149], v25 offset:8192\n"
    "ds_read_b64_tr_b16 v[150:151], v25 offset:9216\n"
    "buffer_load_dwordx4 v11, s[12:15], s75 offen offset:1024 lds\n"
    "v_mfma_f32_32x32x16_bf16 v[80:95], a[228:231], v[128:131], v[80:95]\n"
    "ds_read_b128 a[244:247], v40 offset:4096\n"
    "s_waitcnt lgkmcnt(6)\n"
    "v_mfma_f32_32x32x16_bf16 v[64:79], a[232:235], v[136:139], v[64:79]\n"
    "ds_read_b128 a[248:251], v41 offset:0\n"
    "ds_read_b64_tr_b16 v[152:153], v24 offset:12288\n"
    "buffer_load_dwordx4 v12, s[12:15], s75 offen offset:2048 lds\n"
    "v_mfma_f32_32x32x16_bf16 v[96:111], a[232:235], v[140:143], v[96:111]\n"
    "ds_read_b64_tr_b16 v[154:155], v24 offset:13312\n"
    "v_mfma_f32_32x32x16_bf16 v[112:127], a[236:239], v[140:143], v[112:127]\n"
    "ds_read_b64_tr_b16 v[156:157], v25 offset:12288\n"
    "ds_read_b64_tr_b16 v[158:159], v25 offset:13312\n"
    "buffer_load_dwordx4 v13, s[12:15], s75 offen offset:3072 lds\n"
    "v_mfma_f32_32x32x16_bf16 v[80:95], a[236:239], v[136:139], v[80:95]\n"
    "ds_read_b128 a[252:255], v41 offset:4096\n"
    "s_waitcnt lgkmcnt(6)\n"
    "v_mfma_f32_32x32x16_bf16 v[64:79], a[240:243], v[144:147], v[64:79]\n"
    "s_lshl_b32 s4, s70, 2\n"
    "s_add_u32 s4, s4, s65\n"
    "s_lshl_b32 s4, s4, 12\n"
    "v_and_b32 v2, 31, v1\n"
    "v_lshl_add_u32 v2, v2, 7, s4\n"
    "global_load_dword v62, v2, s[56:57]\n"
    "v_mfma_f32_32x32x16_bf16 v[80:95], a[244:247], v[144:147], v[80:95]\n"
    "s_waitcnt lgkmcnt(0)\n"
    "v_mfma_f32_32x32x16_bf16 v[64:79], a[248:251], v[152:155], v[64:79]\n"
    "v_mfma_f32_32x32x16_bf16 v[80:95], a[252:255], v[152:155], v[80:95]\n"
    "v_mfma_f32_32x32x16_bf16 v[96:111], a[240:243], v[148:151], v[96:111]\n"
    "v_mfma_f32_32x32x16_bf16 v[96:111], a[248:251], v[156:159], v[96:111]\n"
    "s_nop 8\n"
    "v_pk_fma_f32 v[52:53], v[64:65], s[82:83], s[82:83] op_sel:[0,0,1] op_sel_hi:[1,0,1]\n"
    "v_pk_fma_f32 v[54:55], v[66:67], s[82:83], s[82:83] op_sel:[0,0,1] op_sel_hi:[1,0,1]\n"
    "v_pk_fma_f32 v[56:57], v[68:69], s[82:83], s[82:83] op_sel:[0,0,1] op_sel_hi:[1,0,1]\n"
    "v_pk_fma_f32 v[58:59], v[70:71], s[82:83], s[82:83] op_sel:[0,0,1] op_sel_hi:[1,0,1]\n"
    "v_mfma_f32_32x32x16_bf16 v[112:127], a[244:247], v[148:151], v[112:127]\n"
    "v_exp_f32_e64 v52, v52 clamp\n"
    "v_max_f32 v64, s84, v64\n"
    "v_exp_f32_e64 v53, v53 clamp\n"
    "v_max_f32 v65, s84, v65\n"
    "v_exp_f32_e64 v54, v54 clamp\n"
    "v_max_f32 v66, s84, v66\n"
    "v_exp_f32_e64 v55, v55 clamp\n"
    "v_max_f32 v67, s84, v67\n"
    "v_exp_f32_e64 v56, v56 clamp\n"
    "v_max_f32 v68, s84, v68\n"
    "v_exp_f32_e64 v57, v57 clamp\n"
    "v_max_f32 v69, s84, v69\n"
    "v_mfma_f32_32x32x16_bf16 v[112:127], a[252:255], v[156:159], v[112:127]\n"
    "v_exp_f32_e64 v58, v58 clamp\n"
    "v_max_f32 v70, s84, v70\n"
    "v_exp_f32_e64 v59, v59 clamp\n"
    "v_max_f32 v71, s84, v71\n"
    "v_pk_add_f32 v[64:65], v[64:65], v[52:53]\n"
    "v_pk_add_f32 v[66:67], v[66:67], v[54:55]\n"
    "v_pk_add_f32 v[68:69], v[68:69], v[56:57]\n"
    "v_pk_add_f32 v[70:71], v[70:71], v[58:59]\n"
    "v_cvt_pk_bf16_f32 v160, v64, v65\n"
    "v_cvt_pk_bf16_f32 v161, v66, v67\n"
    "v_cvt_pk_bf16_f32 v162, v68, v69\n"
    "v_cvt_pk_bf16_f32 v163, v70, v71\n"
    "v_pk_fma_f32 v[52:53], v[72:73], s[82:83], s[82:83] op_sel:[0,0,1] op_sel_hi:[1,0,1]\n"
    "v_pk_fma_f32 v[54:55], v[74:75], s[82:83], s[82:83] op_sel:[0,0,1] op_sel_hi:[1,0,1]\n"
    "v_pk_fma_f32 v[56:57], v[76:77], s[82:83], s[82:83] op_sel:[0,0,1] op_sel_hi:[1,0,1]\n"
    "v_pk_fma_f32 v[58:59], v[78:79], s[82:83], s[82:83] op_sel:[0,0,1] op_sel_hi:[1,0,1]\n"
    "v_exp_f32_e64 v52, v52 clamp\n"
    "v_max_f32 v72, s84, v72\n"
    "v_exp_f32_e64 v53, v53 clamp\n"
    "v_max_f32 v73, s84, v73\n"
    "v_exp_f32_e64 v54, v54 clamp\n"
    "v_max_f32 v74, s84, v74\n"
    "v_exp_f32_e64 v55, v55 clamp\n"
    "v_max_f32 v75, s84, v75\n"
    "v_exp_f32_e64 v56, v56 clamp\n"
    "v_max_f32 v76, s84, v76\n"
    "v_exp_f32_e64 v57, v57 clamp\n"
    "v_max_f32 v77, s84, v77\n"
    "v_exp_f32_e64 v58, v58 clamp\n"
    "v_max_f32 v78, s84, v78\n"
    "v_exp_f32_e64 v59, v59 clamp\n"
    "v_max_f32 v79, s84, v79\n"
    "v_pk_add_f32 v[72:73], v[72:73], v[52:53]\n"
    "v_pk_add_f32 v[74:75], v[74:75], v[54:55]\n"
    "v_pk_add_f32 v[76:77], v[76:77], v[56:57]\n"
    "v_pk_add_f32 v[78:79], v[78:79], v[58:59]\n"
    "v_cvt_pk_bf16_f32 v164, v72, v73\n"
    "v_cvt_pk_bf16_f32 v165, v74, v75\n"
    "v_cvt_pk_bf16_f32 v166, v76, v77\n"
    "v_cvt_pk_bf16_f32 v167, v78, v79\n"
    "v_cvt_pk_bf16_f32 v176, v96, v97\n"
    "v_cvt_pk_bf16_f32 v177, v98, v99\n"
    "v_cvt_pk_bf16_f32 v178, v100, v101\n"
    "v_cvt_pk_bf16_f32 v179, v102, v103\n"
    "v_cvt_pk_bf16_f32 v180, v104, v105\n"
    "v_cvt_pk_bf16_f32 v181, v106, v107\n"
    "v_cvt_pk_bf16_f32 v182, v108, v109\n"
    "v_cvt_pk_bf16_f32 v183, v110, v111\n"
    "v_mfma_f32_32x32x16_bf16 v[144:159], v[160:163], v[34:37], 0\n"
    "v_mfma_f32_32x32x16_bf16 v[144:159], v[164:167], v[34:37], v[144:159]\n"
    "v_mfma_f32_32x32x16_bf16 v[128:143], v[160:163], v[176:179], 0\n"
    "v_mfma_f32_32x32x16_bf16 v[128:143], v[164:167], v[180:183], v[128:143]\n"
    "v_pk_fma_f32 v[52:53], v[80:81], s[82:83], s[82:83] op_sel:[0,0,1] op_sel_hi:[1,0,1]\n"
    "v_pk_fma_f32 v[54:55], v[82:83], s[82:83], s[82:83] op_sel:[0,0,1] op_sel_hi:[1,0,1]\n"
    "v_pk_fma_f32 v[56:57], v[84:85], s[82:83], s[82:83] op_sel:[0,0,1] op_sel_hi:[1,0,1]\n"
    "v_pk_fma_f32 v[58:59], v[86:87], s[82:83], s[82:83] op_sel:[0,0,1] op_sel_hi:[1,0,1]\n"
    "v_exp_f32_e64 v52, v52 clamp\n"
    "v_max_f32 v80, s84, v80\n"
    "v_exp_f32_e64 v53, v53 clamp\n"
    "v_max_f32 v81, s84, v81\n"
    "v_exp_f32_e64 v54, v54 clamp\n"
    "v_max_f32 v82, s84, v82\n"
    "v_exp_f32_e64 v55, v55 clamp\n"
    "v_max_f32 v83, s84, v83\n"
    "v_exp_f32_e64 v56, v56 clamp\n"
    "v_max_f32 v84, s84, v84\n"
    "v_exp_f32_e64 v57, v57 clamp\n"
    "v_max_f32 v85, s84, v85\n"
    "v_exp_f32_e64 v58, v58 clamp\n"
    "v_max_f32 v86, s84, v86\n"
    "v_exp_f32_e64 v59, v59 clamp\n"
    "v_max_f32 v87, s84, v87\n"
    "v_pk_add_f32 v[80:81], v[80:81], v[52:53]\n"
    "v_pk_add_f32 v[82:83], v[82:83], v[54:55]\n"
    "v_pk_add_f32 v[84:85], v[84:85], v[56:57]\n"
    "v_pk_add_f32 v[86:87], v[86:87], v[58:59]\n"
    "v_cvt_pk_bf16_f32 v168, v80, v81\n"
    "v_cvt_pk_bf16_f32 v169, v82, v83\n"
    "v_cvt_pk_bf16_f32 v170, v84, v85\n"
    "v_cvt_pk_bf16_f32 v171, v86, v87\n"
    "v_pk_fma_f32 v[52:53], v[88:89], s[82:83], s[82:83] op_sel:[0,0,1] op_sel_hi:[1,0,1]\n"
    "v_pk_fma_f32 v[54:55], v[90:91], s[82:83], s[82:83] op_sel:[0,0,1] op_sel_hi:[1,0,1]\n"
    "v_pk_fma_f32 v[56:57], v[92:93], s[82:83], s[82:83] op_sel:[0,0,1] op_sel_hi:[1,0,1]\n"
    "v_pk_fma_f32 v[58:59], v[94:95], s[82:83], s[82:83] op_sel:[0,0,1] op_sel_hi:[1,0,1]\n"
    "v_exp_f32_e64 v52, v52 clamp\n"
    "v_max_f32 v88, s84, v88\n"
    "v_exp_f32_e64 v53, v53 clamp\n"
    "v_max_f32 v89, s84, v89\n"
    "v_exp_f32_e64 v54, v54 clamp\n"
    "v_max_f32 v90, s84, v90\n"
    "v_exp_f32_e64 v55, v55 clamp\n"
    "v_max_f32 v91, s84, v91\n"
    "v_exp_f32_e64 v56, v56 clamp\n"
    "v_max_f32 v92, s84, v92\n"
    "v_exp_f32_e64 v57, v57 clamp\n"
    "v_max_f32 v93, s84, v93\n"
    "v_exp_f32_e64 v58, v58 clamp\n"
    "v_max_f32 v94, s84, v94\n"
    "v_exp_f32_e64 v59, v59 clamp\n"
    "v_max_f32 v95, s84, v95\n"
    "v_pk_add_f32 v[88:89], v[88:89], v[52:53]\n"
    "v_pk_add_f32 v[90:91], v[90:91], v[54:55]\n"
    "v_pk_add_f32 v[92:93], v[92:93], v[56:57]\n"
    "v_pk_add_f32 v[94:95], v[94:95], v[58:59]\n"
    "v_cvt_pk_bf16_f32 v172, v88, v89\n"
    "v_cvt_pk_bf16_f32 v173, v90, v91\n"
    "v_cvt_pk_bf16_f32 v174, v92, v93\n"
    "v_cvt_pk_bf16_f32 v175, v94, v95\n"
    "v_cvt_pk_bf16_f32 v184, v112, v113\n"
    "v_cvt_pk_bf16_f32 v185, v114, v115\n"
    "v_cvt_pk_bf16_f32 v186, v116, v117\n"
    "v_cvt_pk_bf16_f32 v187, v118, v119\n"
    "v_cvt_pk_bf16_f32 v188, v120, v121\n"
    "v_cvt_pk_bf16_f32 v189, v122, v123\n"
    "v_cvt_pk_bf16_f32 v190, v124, v125\n"
    "v_cvt_pk_bf16_f32 v191, v126, v127\n"
    "v_mfma_f32_32x32x16_bf16 v[144:159], v[168:171], v[34:37], v[144:159]\n"
    "v_mfma_f32_32x32x16_bf16 v[144:159], v[172:175], v[34:37], v[144:159]\n"
    "v_mfma_f32_32x32x16_bf16 v[128:143], v[168:171], v[184:187], v[128:143]\n"
    "v_mfma_f32_32x32x16_bf16 v[128:143], v[172:175], v[188:191], v[128:143]\n"
    "s_nop 9\n"
    "v_cndmask_b32 v72, v144, v152, s[86:87]\n"
    "v_cndmask_b32 v73, v145, v153, s[86:87]\n"
    "v_cndmask_b32 v74, v146, v154, s[86:87]\n"
    "v_cndmask_b32 v75, v147, v155, s[86:87]\n"
    "v_cndmask_b32 v76, v148, v156, s[86:87]\n"
    "v_cndmask_b32 v77, v149, v157, s[86:87]\n"
    "v_cndmask_b32 v78, v150, v158, s[86:87]\n"
    "v_cndmask_b32 v79, v151, v159, s[86:87]\n"
    "v_cndmask_b32 v64, v128, v136, s[86:87]\n"
    "v_cndmask_b32 v65, v129, v137, s[86:87]\n"
    "v_cndmask_b32 v66, v130, v138, s[86:87]\n"
    "v_cndmask_b32 v67, v131, v139, s[86:87]\n"
    "v_cndmask_b32 v68, v132, v140, s[86:87]\n"
    "v_cndmask_b32 v69, v133, v141, s[86:87]\n"
    "v_cndmask_b32 v70, v134, v142, s[86:87]\n"
    "v_cndmask_b32 v71, v135, v143, s[86:87]\n"
    "s_cmp_eq_u32 s66, 0\n"
    "s_cbranch_scc1 Lw1_12%=\n"
    "ds_write_b128 v43, v[72:75] offset:2048\n"
    "ds_write_b128 v43, v[76:79] offset:3072\n"
    "ds_write_b128 v43, v[64:67] offset:0\n"
    "ds_write_b128 v43, v[68:71] offset:1024\n"
    "s_waitcnt lgkmcnt(0)\n"
    "s_barrier\n"
    "s_waitcnt vmcnt(1)\n"
    "s_branch Lw2_13%=\n"
    "Lw1_12%=:\n"
    "s_barrier\n"
    "ds_read_b128 v[80:83], v43 offset:0\n"
    "ds_read_b128 v[84:87], v43 offset:1024\n"
    "ds_read_b128 v[88:91], v43 offset:2048\n"
    "ds_read_b128 v[92:95], v43 offset:3072\n"
    "s_waitcnt lgkmcnt(2)\n"
    "v_pk_add_f32 v[64:65], v[64:65], v[80:81]\n"
    "v_pk_add_f32 v[66:67], v[66:67], v[82:83]\n"
    "v_pk_add_f32 v[68:69], v[68:69], v[84:85]\n"
    "v_pk_add_f32 v[70:71], v[70:71], v[86:87]\n"
    "v_cvt_pk_bf16_f32 v96, v64, v65\n"
    "v_cvt_pk_bf16_f32 v97, v66, v67\n"
    "v_cvt_pk_bf16_f32 v98, v68, v69\n"
    "v_cvt_pk_bf16_f32 v99, v70, v71\n"
    "buffer_store_dwordx4 v[96:99], v44, s[20:23], 0 offen sc1\n"
    "s_waitcnt lgkmcnt(0)\n"
    "v_pk_add_f32 v[72:73], v[72:73], v[88:89]\n"
    "v_pk_add_f32 v[74:75], v[74:75], v[90:91]\n"
    "v_pk_add_f32 v[76:77], v[76:77], v[92:93]\n"
    "v_pk_add_f32 v[78:79], v[78:79], v[94:95]\n"
    "v_cvt_pk_bf16_f32 v100, v72, v73\n"
    "v_cvt_pk_bf16_f32 v101, v74, v75\n"
    "v_cvt_pk_bf16_f32 v102, v76, v77\n"
    "v_cvt_pk_bf16_f32 v103, v78, v79\n"
    "buffer_store_dwordx4 v[100:103], v45, s[20:23], 0 offen sc1\n"
    "s_waitcnt vmcnt(3)\n"
    "Lw2_13%=:\n"
    "s_barrier\n"
    "ds_read_b64_tr_b16 v[160:161], v42 offset:0\n"
    "ds_read_b64_tr_b16 v[162:163], v42 offset:512\n"
    "ds_read_b64_tr_b16 v[164:165], v42 offset:2048\n"
    "ds_read_b64_tr_b16 v[166:167], v42 offset:2560\n"
    "ds_read_b64_tr_b16 v[168:169], v42 offset:4096\n"
    "ds_read_b64_tr_b16 v[170:171], v42 offset:4608\n"
    "s_waitcnt lgkmcnt(4)\n"
    "v_mfma_f32_32x32x16_bf16 v[192:207], v[160:163], a[0:3], v[192:207]\n"
    "v_mfma_f32_32x32x16_bf16 v[208:223], v[160:163], a[4:7], v[208:223]\n"
    "ds_read_b64_tr_b16 v[172:173], v42 offset:6144\n"
    "ds_read_b64_tr_b16 v[174:175], v42 offset:6656\n"
    "s_waitcnt lgkmcnt(4)\n"
    "v_mfma_f32_32x32x16_bf16 v[192:207], v[164:167], a[8:11], v[192:207]\n"
    "v_mfma_f32_32x32x16_bf16 v[208:223], v[164:167], a[12:15], v[208:223]\n"
    "ds_read_b64_tr_b16 v[160:161], v42 offset:8192\n"
    "ds_read_b64_tr_b16 v[162:163], v42 offset:8704\n"
    "s_waitcnt lgkmcnt(4)\n"
    "v_mfma_f32_32x32x16_bf16 v[192:207], v[168:171], a[16:19], v[192:207]\n"
    "v_mfma_f32_32x32x16_bf16 v[208:223], v[168:171], a[20:23], v[208:223]\n"
    "ds_read_b64_tr_b16 v[164:165], v42 offset:10240\n"
    "ds_read_b64_tr_b16 v[166:167], v42 offset:10752\n"
    "s_waitcnt lgkmcnt(4)\n"
    "v_mfma_f32_32x32x16_bf16 v[192:207], v[172:175], a[24:27], v[192:207]\n"
    "v_mfma_f32_32x32x16_bf16 v[208:223], v[172:175], a[28:31], v[208:223]\n"
    "ds_read_b64_tr_b16 v[168:169], v42 offset:12288\n"
    "ds_read_b64_tr_b16 v[170:171], v42 offset:12800\n"
    "s_waitcnt lgkmcnt(4)\n"
    "v_mfma_f32_32x32x16_bf16 v[192:207], v[160:163], a[32:35], v[192:207]\n"
    "v_mfma_f32_32x32x16_bf16 v[208:223], v[160:163], a[36:39], v[208:223]\n"
    "ds_read_b64_tr_b16 v[172:173], v42 offset:14336\n"
    "ds_read_b64_tr_b16 v[174:175], v42 offset:14848\n"
    "s_waitcnt lgkmcnt(4)\n"
    "v_mfma_f32_32x32x16_bf16 v[192:207], v[164:167], a[40:43], v[192:207]\n"
    "v_mfma_f32_32x32x16_bf16 v[208:223], v[164:167], a[44:47], v[208:223]\n"
    "ds_read_b64_tr_b16 v[160:161], v42 offset:16384\n"
    "ds_read_b64_tr_b16 v[162:163], v42 offset:16896\n"
    "s_waitcnt lgkmcnt(4)\n"
    "v_mfma_f32_32x32x16_bf16 v[192:207], v[168:171], a[48:51], v[192:207]\n"
    "s_waitcnt vmcnt(0)\n"
    "buffer_store_dwordx2 v[50:51], off, s[20:23], s16 sc1\n"
    "v_mfma_f32_32x32x16_bf16 v[208:223], v[168:171], a[52:55], v[208:223]\n"
    "ds_read_b64_tr_b16 v[164:165], v42 offset:18432\n"
    "ds_read_b64_tr_b16 v[166:167], v42 offset:18944\n"
    "s_waitcnt lgkmcnt(4)\n"
    "v_mfma_f32_32x32x16_bf16 v[192:207], v[172:175], a[56:59], v[192:207]\n"
    "s_add_u32 m0, s77, 65536\n"
    "s_mov_b32 s4, 262144\n"
    "buffer_load_dwordx4 v14, s[28:31], s4 offen offset:0 lds\n"
    "v_mfma_f32_32x32x16_bf16 v[208:223], v[172:175], a[60:63], v[208:223]\n"
    "ds_read_b64_tr_b16 v[168:169], v42 offset:20480\n"
    "ds_read_b64_tr_b16 v[170:171], v42 offset:20992\n"
    "s_waitcnt lgkmcnt(4)\n"
    "v_mfma_f32_32x32x16_bf16 v[192:207], v[160:163], a[64:67], v[192:207]\n"
    "buffer_load_dwordx4 v15, s[28:31], s4 offen offset:1024 lds\n"
    "v_mfma_f32_32x32x16_bf16 v[208:223], v[160:163], a[68:71], v[208:223]\n"
    "ds_read_b64_tr_b16 v[172:173], v42 offset:22528\n"
    "ds_read_b64_tr_b16 v[174:175], v42 offset:23040\n"
    "s_waitcnt lgkmcnt(4)\n"
    "v_mfma_f32_32x32x16_bf16 v[192:207], v[164:167], a[72:75], v[192:207]\n"
    "buffer_load_dwordx4 v16, s[28:31], s4 offen offset:2048 lds\n"
    "v_mfma_f32_32x32x16_bf16 v[208:223], v[164:167], a[76:79], v[208:223]\n"
    "ds_read_b64_tr_b16 v[160:161], v42 offset:24576\n"
    "ds_read_b64_tr_b16 v[162:163], v42 offset:25088\n"
    "s_waitcnt lgkmcnt(4)\n"
    "v_mfma_f32_32x32x16_bf16 v[192:207], v[168:171], a[80:83], v[192:207]\n"
    "buffer_load_dwordx4 v17, s[28:31], s4 offen offset:3072 lds\n"
    "v_mfma_f32_32x32x16_bf16 v[208:223], v[168:171], a[84:87], v[208:223]\n"
    "ds_read_b64_tr_b16 v[164:165], v42 offset:26624\n"
    "ds_read_b64_tr_b16 v[166:167], v42 offset:27136\n"
    "s_waitcnt lgkmcnt(4)\n"
    "v_mfma_f32_32x32x16_bf16 v[192:207], v[172:175], a[88:91], v[192:207]\n"
    "s_add_u32 m0, s77, 81920\n"
    "s_mov_b32 s4, 327680\n"
    "buffer_load_dwordx4 v14, s[28:31], s4 offen offset:0 lds\n"
    "v_mfma_f32_32x32x16_bf16 v[208:223], v[172:175], a[92:95], v[208:223]\n"
    "ds_read_b64_tr_b16 v[168:169], v42 offset:28672\n"
    "ds_read_b64_tr_b16 v[170:171], v42 offset:29184\n"
    "s_waitcnt lgkmcnt(4)\n"
    "v_mfma_f32_32x32x16_bf16 v[192:207], v[160:163], a[96:99], v[192:207]\n"
    "buffer_load_dwordx4 v15, s[28:31], s4 offen offset:1024 lds\n"
    "v_mfma_f32_32x32x16_bf16 v[208:223], v[160:163], a[100:103], v[208:223]\n"
    "ds_read_b64_tr_b16 v[172:173], v42 offset:30720\n"
    "ds_read_b64_tr_b16 v[174:175], v42 offset:31232\n"
    "s_lshl_b32 s7, s78, 4\n"
    "s_waitcnt lgkmcnt(4)\n"
    "v_mfma_f32_32x32x16_bf16 v[192:207], v[164:167], a[104:107], v[192:207]\n"
    "buffer_load_dwordx4 v16, s[28:31], s4 offen offset:2048 lds\n"
    "v_mfma_f32_32x32x16_bf16 v[208:223], v[164:167], a[108:111], v[208:223]\n"
    "ds_read_b64_tr_b16 v[160:161], v42 offset:32768\n"
    "ds_read_b64_tr_b16 v[162:163], v42 offset:33280\n"
    "s_lshl_b32 s5, s66, 3\n"
    "s_waitcnt lgkmcnt(4)\n"
    "v_mfma_f32_32x32x16_bf16 v[192:207], v[168:171], a[112:115], v[192:207]\n"
    "buffer_load_dwordx4 v17, s[28:31], s4 offen offset:3072 lds\n"
    "v_mfma_f32_32x32x16_bf16 v[208:223], v[168:171], a[116:119], v[208:223]\n"
    "ds_read_b64_tr_b16 v[164:165], v42 offset:34816\n"
    "ds_read_b64_tr_b16 v[166:167], v42 offset:35328\n"
    "s_add_u32 s7, s7, s5\n"
    "s_waitcnt lgkmcnt(4)\n"
    "v_mfma_f32_32x32x16_bf16 v[192:207], v[172:175], a[120:123], v[192:207]\n"
    "s_add_u32 m0, s77, 98304\n"
    "s_mov_b32 s4, 393216\n"
    "buffer_load_dwordx4 v14, s[28:31], s4 offen offset:0 lds\n"
    "v_mfma_f32_32x32x16_bf16 v[208:223], v[172:175], a[124:127], v[208:223]\n"
    "ds_read_b64_tr_b16 v[168:169], v42 offset:36864\n"
    "ds_read_b64_tr_b16 v[170:171], v42 offset:37376\n"
    "s_lshl_b32 s5, s7, 10\n"
    "s_waitcnt lgkmcnt(4)\n"
    "v_mfma_f32_32x32x16_bf16 v[192:207], v[160:163], a[128:131], v[192:207]\n"
    "buffer_load_dwordx4 v15, s[28:31], s4 offen offset:1024 lds\n"
    "v_mfma_f32_32x32x16_bf16 v[208:223], v[160:163], a[132:135], v[208:223]\n"
    "ds_read_b64_tr_b16 v[172:173], v42 offset:38912\n"
    "ds_read_b64_tr_b16 v[174:175], v42 offset:39424\n"
    "s_add_u32 s5, s5, 262144\n"
    "s_waitcnt lgkmcnt(4)\n"
    "v_mfma_f32_32x32x16_bf16 v[192:207], v[164:167], a[136:139], v[192:207]\n"
    "buffer_load_dwordx4 v16, s[28:31], s4 offen offset:2048 lds\n"
    "v_mfma_f32_32x32x16_bf16 v[208:223], v[164:167], a[140:143], v[208:223]\n"
    "ds_read_b64_tr_b16 v[160:161], v42 offset:40960\n"
    "ds_read_b64_tr_b16 v[162:163], v42 offset:41472\n"
    "v_lshl_add_u32 v3, v1, 4, s5\n"
    "s_waitcnt lgkmcnt(4)\n"
    "v_mfma_f32_32x32x16_bf16 v[192:207], v[168:171], a[144:147], v[192:207]\n"
    "buffer_load_dwordx4 v17, s[28:31], s4 offen offset:3072 lds\n"
    "v_mfma_f32_32x32x16_bf16 v[208:223], v[168:171], a[148:151], v[208:223]\n"
    "ds_read_b64_tr_b16 v[164:165], v42 offset:43008\n"
    "ds_read_b64_tr_b16 v[166:167], v42 offset:43520\n"
    "s_lshl_b32 s5, s7, 6\n"
    "s_waitcnt lgkmcnt(4)\n"
    "v_mfma_f32_32x32x16_bf16 v[192:207], v[172:175], a[152:155], v[192:207]\n"
    "s_add_u32 m0, s77, 114688\n"
    "s_mov_b32 s4, 458752\n"
    "buffer_load_dwordx4 v14, s[28:31], s4 offen offset:0 lds\n"
    "v_mfma_f32_32x32x16_bf16 v[208:223], v[172:175], a[156:159], v[208:223]\n"
    "ds_read_b64_tr_b16 v[168:169], v42 offset:45056\n"
    "ds_read_b64_tr_b16 v[170:171], v42 offset:45568\n"
    "s_add_u32 s5, s5, 32768\n"
    "s_waitcnt lgkmcnt(4)\n"
    "v_mfma_f32_32x32x16_bf16 v[192:207], v[160:163], a[160:163], v[192:207]\n"
    "buffer_load_dwordx4 v15, s[28:31], s4 offen offset:1024 lds\n"
    "v_mfma_f32_32x32x16_bf16 v[208:223], v[160:163], a[164:167], v[208:223]\n"
    "ds_read_b64_tr_b16 v[172:173], v42 offset:47104\n"
    "ds_read_b64_tr_b16 v[174:175], v42 offset:47616\n"
    "v_add_u32 v6, s5, v46\n"
    "s_waitcnt lgkmcnt(4)\n"
    "v_mfma_f32_32x32x16_bf16 v[192:207], v[164:167], a[168:171], v[192:207]\n"
    "buffer_load_dwordx4 v16, s[28:31], s4 offen offset:2048 lds\n"
    "buffer_load_dwordx4 v17, s[28:31], s4 offen offset:3072 lds\n"
    "v_mfma_f32_32x32x16_bf16 v[208:223], v[164:167], a[172:175], v[208:223]\n"
    "ds_read_b64_tr_b16 v[160:161], v42 offset:49152\n"
    "ds_read_b64_tr_b16 v[162:163], v42 offset:49664\n"
    "s_waitcnt lgkmcnt(4)\n"
    "v_mfma_f32_32x32x16_bf16 v[192:207], v[168:171], a[176:179], v[192:207]\n"
    "v_mfma_f32_32x32x16_bf16 v[208:223], v[168:171], a[180:183], v[208:223]\n"
    "ds_read_b64_tr_b16 v[164:165], v42 offset:51200\n"
    "ds_read_b64_tr_b16 v[166:167], v42 offset:51712\n"
    "s_waitcnt lgkmcnt(4)\n"
    "v_mfma_f32_32x32x16_bf16 v[192:207], v[172:175], a[184:187], v[192:207]\n"
    "buffer_load_dwordx2 v[8:9], v47, s[20:23], 0 offen sc1\n"
    "v_mfma_f32_32x32x16_bf16 v[208:223], v[172:175], a[188:191], v[208:223]\n"
    "ds_read_b64_tr_b16 v[168:169], v42 offset:53248\n"
    "ds_read_b64_tr_b16 v[170:171], v42 offset:53760\n"
    "s_waitcnt lgkmcnt(4)\n"
    "v_mfma_f32_32x32x16_bf16 v[192:207], v[160:163], a[192:195], v[192:207]\n"
    "v_mfma_f32_32x32x16_bf16 v[208:223], v[160:163], a[196:199], v[208:223]\n"
    "ds_read_b64_tr_b16 v[172:173], v42 offset:55296\n"
    "ds_read_b64_tr_b16 v[174:175], v42 offset:55808\n"
    "s_waitcnt lgkmcnt(4)\n"
    "v_mfma_f32_32x32x16_bf16 v[192:207], v[164:167], a[200:203], v[192:207]\n"
    "v_mfma_f32_32x32x16_bf16 v[208:223], v[164:167], a[204:207], v[208:223]\n"
    "ds_read_b64_tr_b16 v[160:161], v42 offset:57344\n"
    "ds_read_b64_tr_b16 v[162:163], v42 offset:57856\n"
    "s_waitcnt lgkmcnt(4)\n"
    "v_mfma_f32_32x32x16_bf16 v[192:207], v[168:171], a[208:211], v[192:207]\n"
    "v_mfma_f32_32x32x16_bf16 v[208:223], v[168:171], a[212:215], v[208:223]\n"
    "ds_read_b64_tr_b16 v[164:165], v42 offset:59392\n"
    "ds_read_b64_tr_b16 v[166:167], v42 offset:59904\n"
    "s_waitcnt lgkmcnt(4)\n"
    "v_mfma_f32_32x32x16_bf16 v[192:207], v[172:175], a[216:219], v[192:207]\n"
    "v_mfma_f32_32x32x16_bf16 v[208:223], v[172:175], a[220:223], v[208:223]\n"
    "ds_read_b64_tr_b16 v[168:169], v42 offset:61440\n"
    "ds_read_b64_tr_b16 v[170:171], v42 offset:61952\n"
    "s_waitcnt lgkmcnt(4)\n"
    "v_mfma_f32_32x32x16_bf16 v[192:207], v[160:163], a[224:227], v[192:207]\n"
    "s_waitcnt vmcnt(0)\n"
    "v_cmp_ne_u64 vcc, s[80:81], v[8:9]\n"
    "s_nop 0\n"
    "s_cmp_eq_u64 vcc, 0\n"
    "s_cselect_b32 s5, 0, 2146435072\n"
    "s_add_u32 s6, s5, 4096\n"
    "v_mfma_f32_32x32x16_bf16 v[208:223], v[160:163], a[228:231], v[208:223]\n"
    "ds_read_b64_tr_b16 v[172:173], v42 offset:63488\n"
    "ds_read_b64_tr_b16 v[174:175], v42 offset:64000\n"
    "buffer_load_dwordx4 v[64:67], v6, s[20:23], s5 offen sc1\n"
    "buffer_load_dwordx4 v[128:131], v3, s[20:23], s5 offen offset:0 sc1\n"
    "s_waitcnt lgkmcnt(4)\n"
    "v_mfma_f32_32x32x16_bf16 v[192:207], v[164:167], a[232:235], v[192:207]\n"
    "buffer_load_dwordx4 v[132:135], v3, s[20:23], s5 offen offset:1024 sc1\n"
    "buffer_load_dwordx4 v[136:139], v3, s[20:23], s5 offen offset:2048 sc1\n"
    "v_mfma_f32_32x32x16_bf16 v[208:223], v[164:167], a[236:239], v[208:223]\n"
    "buffer_load_dwordx4 v[140:143], v3, s[20:23], s5 offen offset:3072 sc1\n"
    "buffer_load_dwordx4 v[144:147], v3, s[20:23], s6 offen offset:0 sc1\n"
    "s_waitcnt lgkmcnt(2)\n"
    "v_mfma_f32_32x32x16_bf16 v[192:207], v[168:171], a[240:243], v[192:207]\n"
    "buffer_load_dwordx4 v[148:151], v3, s[20:23], s6 offen offset:1024 sc1\n"
    "buffer_load_dwordx4 v[152:155], v3, s[20:23], s6 offen offset:2048 sc1\n"
    "v_mfma_f32_32x32x16_bf16 v[208:223], v[168:171], a[244:247], v[208:223]\n"
    "buffer_load_dwordx4 v[156:159], v3, s[20:23], s6 offen offset:3072 sc1\n"
    "s_waitcnt lgkmcnt(0)\n"
    "v_mfma_f32_32x32x16_bf16 v[192:207], v[172:175], a[248:251], v[192:207]\n"
    "v_mfma_f32_32x32x16_bf16 v[208:223], v[172:175], a[252:255], v[208:223]\n"
    "s_nop 9\n"
    "s_cmp_eq_u32 s5, 0\n"
    "s_cbranch_scc1 Lok_14%=\n"
    "s_mov_b32 s79, 65536\n"
    "Lpoll_15%=:\n"
    "buffer_load_dwordx2 v[8:9], v47, s[20:23], 0 offen sc1\n"
    "s_sub_u32 s79, s79, 1\n"
    "s_cbranch_scc1 Lpdone_16%=\n"
    "s_waitcnt vmcnt(0)\n"
    "v_cmp_ne_u64 vcc, s[80:81], v[8:9]\n"
    "s_nop 0\n"
    "s_cbranch_vccnz Lpoll_15%=\n"
    "Lpdone_16%=:\n"
    "s_waitcnt vmcnt(0)\n"
    "s_mov_b32 s5, 0\n"
    "s_movk_i32 s6, 4096\n"
    "buffer_load_dwordx4 v[64:67], v6, s[20:23], s5 offen sc1\n"
    "buffer_load_dwordx4 v[128:131], v3, s[20:23], s5 offen offset:0 sc1\n"
    "buffer_load_dwordx4 v[132:135], v3, s[20:23], s5 offen offset:1024 sc1\n"
    "buffer_load_dwordx4 v[136:139], v3, s[20:23], s5 offen offset:2048 sc1\n"
    "buffer_load_dwordx4 v[140:143], v3, s[20:23], s5 offen offset:3072 sc1\n"
    "buffer_load_dwordx4 v[144:147], v3, s[20:23], s6 offen offset:0 sc1\n"
    "buffer_load_dwordx4 v[148:151], v3, s[20:23], s6 offen offset:1024 sc1\n"
    "buffer_load_dwordx4 v[152:155], v3, s[20:23], s6 offen offset:2048 sc1\n"
    "buffer_load_dwordx4 v[156:159], v3, s[20:23], s6 offen offset:3072 sc1\n"
    "Lok_14%=:\n"
    "s_barrier\n"
    "s_add_u32 m0, s77, 0\n"
    "s_mov_b32 s4, 0\n"
    "buffer_load_dwordx4 v14, s[28:31], s4 offen offset:0 lds\n"
    "buffer_load_dwordx4 v15, s[28:31], s4 offen offset:1024 lds\n"
    "buffer_load_dwordx4 v16, s[28:31], s4 offen offset:2048 lds\n"
    "buffer_load_dwordx4 v17, s[28:31], s4 offen offset:3072 lds\n"
    "s_add_u32 m0, s77, 16384\n"
    "s_mov_b32 s4, 65536\n"
    "buffer_load_dwordx4 v14, s[28:31], s4 offen offset:0 lds\n"
    "buffer_load_dwordx4 v15, s[28:31], s4 offen offset:1024 lds\n"
    "buffer_load_dwordx4 v16, s[28:31], s4 offen offset:2048 lds\n"
    "buffer_load_dwordx4 v17, s[28:31], s4 offen offset:3072 lds\n"
    "v_pk_fma_f32 v[52:53], v[192:193], s[82:83], s[82:83] op_sel:[0,0,1] op_sel_hi:[1,0,1]\n"
    "v_pk_fma_f32 v[54:55], v[194:195], s[82:83], s[82:83] op_sel:[0,0,1] op_sel_hi:[1,0,1]\n"
    "v_pk_fma_f32 v[56:57], v[196:197], s[82:83], s[82:83] op_sel:[0,0,1] op_sel_hi:[1,0,1]\n"
    "v_pk_fma_f32 v[58:59], v[198:199], s[82:83], s[82:83] op_sel:[0,0,1] op_sel_hi:[1,0,1]\n"
    "v_exp_f32_e64 v52, v52 clamp\n"
    "v_max_f32 v192, s84, v192\n"
    "v_exp_f32_e64 v53, v53 clamp\n"
    "v_max_f32 v193, s84, v193\n"
    "v_exp_f32_e64 v54, v54 clamp\n"
    "v_max_f32 v194, s84, v194\n"
    "v_exp_f32_e64 v55, v55 clamp\n"
    "v_max_f32 v195, s84, v195\n"
    "v_exp_f32_e64 v56, v56 clamp\n"
    "v_max_f32 v196, s84, v196\n"
    "v_exp_f32_e64 v57, v57 clamp\n"
    "v_max_f32 v197, s84, v197\n"
    "v_exp_f32_e64 v58, v58 clamp\n"
    "v_max_f32 v198, s84, v198\n"
    "v_exp_f32_e64 v59, v59 clamp\n"
    "v_max_f32 v199, s84, v199\n"
    "v_pk_add_f32 v[192:193], v[192:193], v[52:53]\n"
    "v_pk_add_f32 v[194:195], v[194:195], v[54:55]\n"
    "v_pk_add_f32 v[196:197], v[196:197], v[56:57]\n"
    "v_pk_add_f32 v[198:199], v[198:199], v[58:59]\n"
    "v_cvt_pk_bf16_f32 v224, v192, v193\n"
    "v_cvt_pk_bf16_f32 v225, v194, v195\n"
    "v_cvt_pk_bf16_f32 v226, v196, v197\n"
    "v_cvt_pk_bf16_f32 v227, v198, v199\n"
    "v_pk_fma_f32 v[52:53], v[200:201], s[82:83], s[82:83] op_sel:[0,0,1] op_sel_hi:[1,0,1]\n"
    "v_pk_fma_f32 v[54:55], v[202:203], s[82:83], s[82:83] op_sel:[0,0,1] op_sel_hi:[1,0,1]\n"
    "v_pk_fma_f32 v[56:57], v[204:205], s[82:83], s[82:83] op_sel:[0,0,1] op_sel_hi:[1,0,1]\n"
    "v_pk_fma_f32 v[58:59], v[206:207], s[82:83], s[82:83] op_sel:[0,0,1] op_sel_hi:[1,0,1]\n"
    "v_exp_f32_e64 v52, v52 clamp\n"
    "v_max_f32 v200, s84, v200\n"
    "v_exp_f32_e64 v53, v53 clamp\n"
    "v_max_f32 v201, s84, v201\n"
    "v_exp_f32_e64 v54, v54 clamp\n"
    "v_max_f32 v202, s84, v202\n"
    "v_exp_f32_e64 v55, v55 clamp\n"
    "v_max_f32 v203, s84, v203\n"
    "v_exp_f32_e64 v56, v56 clamp\n"
    "v_max_f32 v204, s84, v204\n"
    "v_exp_f32_e64 v57, v57 clamp\n"
    "v_max_f32 v205, s84, v205\n"
    "v_exp_f32_e64 v58, v58 clamp\n"
    "v_max_f32 v206, s84, v206\n"
    "v_exp_f32_e64 v59, v59 clamp\n"
    "v_max_f32 v207, s84, v207\n"
    "v_pk_add_f32 v[200:201], v[200:201], v[52:53]\n"
    "v_pk_add_f32 v[202:203], v[202:203], v[54:55]\n"
    "v_pk_add_f32 v[204:205], v[204:205], v[56:57]\n"
    "v_pk_add_f32 v[206:207], v[206:207], v[58:59]\n"
    "v_cvt_pk_bf16_f32 v228, v200, v201\n"
    "v_cvt_pk_bf16_f32 v229, v202, v203\n"
    "v_cvt_pk_bf16_f32 v230, v204, v205\n"
    "v_cvt_pk_bf16_f32 v231, v206, v207\n"
    "s_waitcnt vmcnt(16)\n"
    "v_lshlrev_b32 v72, 16, v64\n"
    "v_and_b32 v73, 0xffff0000, v64\n"
    "v_lshlrev_b32 v74, 16, v65\n"
    "v_and_b32 v75, 0xffff0000, v65\n"
    "v_lshlrev_b32 v76, 16, v66\n"
    "v_and_b32 v77, 0xffff0000, v66\n"
    "v_lshlrev_b32 v78, 16, v67\n"
    "v_and_b32 v79, 0xffff0000, v67\n"
    "v_add_f32_dpp v72, v72, v72 row_ror:1 row_mask:0xf bank_mask:0xf\n"
    "v_add_f32_dpp v73, v73, v73 row_ror:1 row_mask:0xf bank_mask:0xf\n"
    "v_add_f32_dpp v74, v74, v74 row_ror:1 row_mask:0xf bank_mask:0xf\n"
    "v_add_f32_dpp v75, v75, v75 row_ror:1 row_mask:0xf bank_mask:0xf\n"
    "v_add_f32_dpp v76, v76, v76 row_ror:1 row_mask:0xf bank_mask:0xf\n"
    "v_add_f32_dpp v77, v77, v77 row_ror:1 row_mask:0xf bank_mask:0xf\n"
    "v_add_f32_dpp v78, v78, v78 row_ror:1 row_mask:0xf bank_mask:0xf\n"
    "v_add_f32_dpp v79, v79, v79 row_ror:1 row_mask:0xf bank_mask:0xf\n"
    "v_add_f32_dpp v72, v72, v72 row_ror:2 row_mask:0xf bank_mask:0xf\n"
    "v_add_f32_dpp v73, v73, v73 row_ror:2 row_mask:0xf bank_mask:0xf\n"
    "v_add_f32_dpp v74, v74, v74 row_ror:2 row_mask:0xf bank_mask:0xf\n"
    "v_add_f32_dpp v75, v75, v75 row_ror:2 row_mask:0xf bank_mask:0xf\n"
    "v_add_f32_dpp v76, v76, v76 row_ror:2 row_mask:0xf bank_mask:0xf\n"
    "v_add_f32_dpp v77, v77, v77 row_ror:2 row_mask:0xf bank_mask:0xf\n"
    "v_add_f32_dpp v78, v78, v78 row_ror:2 row_mask:0xf bank_mask:0xf\n"
    "v_add_f32_dpp v79, v79, v79 row_ror:2 row_mask:0xf bank_mask:0xf\n"
    "v_add_f32_dpp v72, v72, v72 row_ror:4 row_mask:0xf bank_mask:0xf\n"
    "v_add_f32_dpp v73, v73, v73 row_ror:4 row_mask:0xf bank_mask:0xf\n"
    "v_add_f32_dpp v74, v74, v74 row_ror:4 row_mask:0xf bank_mask:0xf\n"
    "v_add_f32_dpp v75, v75, v75 row_ror:4 row_mask:0xf bank_mask:0xf\n"
    "v_add_f32_dpp v76, v76, v76 row_ror:4 row_mask:0xf bank_mask:0xf\n"
    "v_add_f32_dpp v77, v77, v77 row_ror:4 row_mask:0xf bank_mask:0xf\n"
    "v_add_f32_dpp v78, v78, v78 row_ror:4 row_mask:0xf bank_mask:0xf\n"
    "v_add_f32_dpp v79, v79, v79 row_ror:4 row_mask:0xf bank_mask:0xf\n"
    "s_waitcnt vmcnt(15)\n"
    "v_mfma_f32_32x32x16_bf16 v[192:207], v[26:29], v[128:131], 0\n"
    "v_pk_fma_f32 v[52:53], v[208:209], s[82:83], s[82:83] op_sel:[0,0,1] op_sel_hi:[1,0,1]\n"
    "v_pk_fma_f32 v[54:55], v[210:211], s[82:83], s[82:83] op_sel:[0,0,1] op_sel_hi:[1,0,1]\n"
    "v_pk_fma_f32 v[56:57], v[212:213], s[82:83], s[82:83] op_sel:[0,0,1] op_sel_hi:[1,0,1]\n"
    "v_pk_fma_f32 v[58:59], v[214:215], s[82:83], s[82:83] op_sel:[0,0,1] op_sel_hi:[1,0,1]\n"
    "v_exp_f32_e64 v52, v52 clamp\n"
    "v_max_f32 v208, s84, v208\n"
    "v_exp_f32_e64 v53, v53 clamp\n"
    "v_max_f32 v209, s84, v209\n"
    "s_waitcnt vmcnt(14)\n"
    "v_mfma_f32_32x32x16_bf16 v[192:207], v[26:29], v[132:135], v[192:207]\n"
    "v_exp_f32_e64 v54, v54 clamp\n"
    "v_max_f32 v210, s84, v210\n"
    "v_exp_f32_e64 v55, v55 clamp\n"
    "v_max_f32 v211, s84, v211\n"
    "v_exp_f32_e64 v56, v56 clamp\n"
    "v_max_f32 v212, s84, v212\n"
    "v_exp_f32_e64 v57, v57 clamp\n"
    "v_max_f32 v213, s84, v213\n"
    "s_waitcnt vmcnt(13)\n"
    "v_mfma_f32_32x32x16_bf16 v[192:207], v[26:29], v[136:139], v[192:207]\n"
    "v_exp_f32_e64 v58, v58 clamp\n"
    "v_max_f32 v214, s84, v214\n"
    "v_exp_f32_e64 v59, v59 clamp\n"
    "v_max_f32 v215, s84, v215\n"
    "v_pk_add_f32 v[208:209], v[208:209], v[52:53]\n"
    "v_pk_add_f32 v[210:211], v[210:211], v[54:55]\n"
    "v_pk_add_f32 v[212:213], v[212:213], v[56:57]\n"
    "v_pk_add_f32 v[214:215], v[214:215], v[58:59]\n"
    "s_waitcnt vmcnt(12)\n"
    "v_mfma_f32_32x32x16_bf16 v[192:207], v[26:29], v[140:143], v[192:207]\n"
    "v_cvt_pk_bf16_f32 v232, v208, v209\n"
    "v_cvt_pk_bf16_f32 v233, v210, v211\n"
    "v_cvt_pk_bf16_f32 v234, v212, v213\n"
    "v_cvt_pk_bf16_f32 v235, v214, v215\n"
    "v_pk_fma_f32 v[52:53], v[216:217], s[82:83], s[82:83] op_sel:[0,0,1] op_sel_hi:[1,0,1]\n"
    "v_pk_fma_f32 v[54:55], v[218:219], s[82:83], s[82:83] op_sel:[0,0,1] op_sel_hi:[1,0,1]\n"
    "v_pk_fma_f32 v[56:57], v[220:221], s[82:83], s[82:83] op_sel:[0,0,1] op_sel_hi:[1,0,1]\n"
    "v_pk_fma_f32 v[58:59], v[222:223], s[82:83], s[82:83] op_sel:[0,0,1] op_sel_hi:[1,0,1]\n"
    "s_waitcnt vmcnt(11)\n"
    "v_mfma_f32_32x32x16_bf16 v[192:207], v[26:29], v[144:147], v[192:207]\n"
    "v_exp_f32_e64 v52, v52 clamp\n"
    "v_max_f32 v216, s84, v216\n"
    "v_exp_f32_e64 v53, v53 clamp\n"
    "v_max_f32 v217, s84, v217\n"
    "v_exp_f32_e64 v54, v54 clamp\n"
    "v_max_f32 v218, s84, v218\n"
    "v_exp_f32_e64 v55, v55 clamp\n"
    "v_max_f32 v219, s84, v219\n"
    "s_waitcnt vmcnt(10)\n"
    "v_mfma_f32_32x32x16_bf16 v[192:207], v[26:29], v[148:151], v[192:207]\n"
    "v_exp_f32_e64 v56, v56 clamp\n"
    "v_max_f32 v220, s84, v220\n"
    "v_exp_f32_e64 v57, v57 clamp\n"
    "v_max_f32 v221, s84, v221\n"
    "v_exp_f32_e64 v58, v58 clamp\n"
    "v_max_f32 v222, s84, v222\n"
    "v_exp_f32_e64 v59, v59 clamp\n"
    "v_max_f32 v223, s84, v223\n"
    "s_waitcnt vmcnt(9)\n"
    "v_mfma_f32_32x32x16_bf16 v[192:207], v[26:29], v[152:155], v[192:207]\n"
    "v_pk_add_f32 v[216:217], v[216:217], v[52:53]\n"
    "v_pk_add_f32 v[218:219], v[218:219], v[54:55]\n"
    "v_pk_add_f32 v[220:221], v[220:221], v[56:57]\n"
    "v_pk_add_f32 v[222:223], v[222:223], v[58:59]\n"
    "v_cvt_pk_bf16_f32 v236, v216, v217\n"
    "v_cvt_pk_bf16_f32 v237, v218, v219\n"
    "v_cvt_pk_bf16_f32 v238, v220, v221\n"
    "v_cvt_pk_bf16_f32 v239, v222, v223\n"
    "s_waitcnt vmcnt(8)\n"
    "v_mfma_f32_32x32x16_bf16 v[192:207], v[26:29], v[156:159], v[192:207]\n"
    "s_lshl_b32 s4, s66, 13\n"
    "v_xor_b32 v2, s4, v43\n"
    "v_xor_b32 v3, 0x2000, v2\n"
    "v_bfe_u32 v4, v1, 2, 2\n"
    "v_xor_b32 v48, s65, v4\n"
    "v_bfe_u32 v5, v1, 4, 1\n"
    "v_lshl_add_u32 v48, v48, 1, v5\n"
    "v_bfe_u32 v5, v1, 1, 1\n"
    "v_lshl_add_u32 v48, v48, 1, v5\n"
    "v_and_b32 v5, 1, v1\n"
    "v_lshl_add_u32 v48, v48, 1, v5\n"
    "v_lshlrev_b32 v48, 3, v48\n"
    "v_lshrrev_b32 v5, 5, v1\n"
    "v_lshl_add_u32 v4, v5, 2, v4\n"
    "v_lshl_add_u32 v52, v4, 8, v48\n"
    "v_add_u32 v53, 0x10000, v52\n"
    "ds_write_b128 v2, v[72:75] offset:2048\n"
    "ds_write_b128 v2, v[76:79] offset:3072\n"
    "ds_write_b128 v2, v[192:195] offset:0\n"
    "ds_write_b128 v2, v[196:199] offset:1024\n"
    "s_waitcnt lgkmcnt(0)\n"
    "s_barrier\n"
    "ds_read_b128 v[216:219], v3 offset:2048\n"
    "ds_read_b128 v[220:223], v3 offset:3072\n"
    "ds_read_b128 v[208:211], v3 offset:0\n"
    "ds_read_b128 v[212:215], v3 offset:1024\n"
    "s_waitcnt lgkmcnt(2)\n"
    "v_pk_add_f32 v[72:73], v[72:73], v[216:217]\n"
    "v_pk_add_f32 v[74:75], v[74:75], v[218:219]\n"
    "v_pk_add_f32 v[76:77], v[76:77], v[220:221]\n"
    "v_pk_add_f32 v[78:79], v[78:79], v[222:223]\n"
    "v_cvt_pk_bf16_f32 v80, v72, v73\n"
    "v_cvt_pk_bf16_f32 v81, v74, v75\n"
    "v_cvt_pk_bf16_f32 v82, v76, v77\n"
    "v_cvt_pk_bf16_f32 v83, v78, v79\n"
    "v_cndmask_b32 v72, v80, 0, s[86:87]\n"
    "v_cndmask_b32 v76, 0, v80, s[86:87]\n"
    "v_cndmask_b32 v73, v81, 0, s[86:87]\n"
    "v_cndmask_b32 v77, 0, v81, s[86:87]\n"
    "v_cndmask_b32 v74, v82, 0, s[86:87]\n"
    "v_cndmask_b32 v78, 0, v82, s[86:87]\n"
    "v_cndmask_b32 v75, v83, 0, s[86:87]\n"
    "v_cndmask_b32 v79, 0, v83, s[86:87]\n"
    "s_nop 0\n"
    "v_mfma_f32_32x32x16_bf16 v[160:175], v[72:75], v[224:227], 0\n"
    "v_mfma_f32_32x32x16_bf16 v[160:175], v[76:79], v[228:231], v[160:175]\n"
    "v_mfma_f32_32x32x16_bf16 v[176:191], v[72:75], v[232:235], 0\n"
    "v_mfma_f32_32x32x16_bf16 v[176:191], v[76:79], v[236:239], v[176:191]\n"
    "s_waitcnt lgkmcnt(0)\n"
    "v_pk_add_f32 v[192:193], v[192:193], v[208:209]\n"
    "v_pk_add_f32 v[194:195], v[194:195], v[210:211]\n"
    "v_pk_add_f32 v[196:197], v[196:197], v[212:213]\n"
    "v_pk_add_f32 v[198:199], v[198:199], v[214:215]\n"
    "v_cvt_pk_bf16_f32 v80, v192, v193\n"
    "v_cvt_pk_bf16_f32 v81, v194, v195\n"
    "v_cvt_pk_bf16_f32 v82, v196, v197\n"
    "v_cvt_pk_bf16_f32 v83, v198, v199\n"
    "v_cndmask_b32 v64, v80, 0, s[86:87]\n"
    "v_cndmask_b32 v68, 0, v80, s[86:87]\n"
    "v_cndmask_b32 v65, v81, 0, s[86:87]\n"
    "v_cndmask_b32 v69, 0, v81, s[86:87]\n"
    "v_cndmask_b32 v66, v82, 0, s[86:87]\n"
    "v_cndmask_b32 v70, 0, v82, s[86:87]\n"
    "v_cndmask_b32 v67, v83, 0, s[86:87]\n"
    "v_cndmask_b32 v71, 0, v83, s[86:87]\n"
    "s_nop 0\n"
    "v_mfma_f32_32x32x16_bf16 v[128:143], v[64:67], v[224:227], 0\n"
    "v_mfma_f32_32x32x16_bf16 v[128:143], v[68:71], v[228:231], v[128:143]\n"
    "v_mfma_f32_32x32x16_bf16 v[144:159], v[64:67], v[232:235], 0\n"
    "v_mfma_f32_32x32x16_bf16 v[144:159], v[68:71], v[236:239], v[144:159]\n"
    "v_add_f32 v160, s84, v160\n"
    "v_add_f32 v168, s84, v168\n"
    "v_add_f32 v176, s84, v176\n"
    "v_add_f32 v184, s84, v184\n"
    "v_rcp_f32 v160, v160\n"
    "v_rcp_f32 v168, v168\n"
    "v_rcp_f32 v176, v176\n"
    "v_rcp_f32 v184, v184\n"
    "s_mov_b32 s4, 0\n"
    "s_mov_b32 s5, 0x10000\n"
    "v_pk_mul_f32 v[128:129], v[128:129], v[160:161] op_sel_hi:[1,0]\n"
    "v_pk_mul_f32 v[130:131], v[130:131], v[160:161] op_sel_hi:[1,0]\n"
    "v_pk_mul_f32 v[132:133], v[132:133], v[160:161] op_sel_hi:[1,0]\n"
    "v_pk_mul_f32 v[134:135], v[134:135], v[160:161] op_sel_hi:[1,0]\n"
    "v_cvt_pk_bf16_f32 v96, v128, v129\n"
    "v_cvt_pk_bf16_f32 v97, v130, v131\n"
    "v_cvt_pk_bf16_f32 v98, v132, v133\n"
    "v_cvt_pk_bf16_f32 v99, v134, v135\n"
    "buffer_store_dwordx4 v[96:99], v49, s[20:23], s4 offen offset:0 sc1\n"
    "v_pk_mul_f32 v[136:137], v[136:137], v[168:169] op_sel_hi:[1,0]\n"
    "v_pk_mul_f32 v[138:139], v[138:139], v[168:169] op_sel_hi:[1,0]\n"
    "v_pk_mul_f32 v[140:141], v[140:141], v[168:169] op_sel_hi:[1,0]\n"
    "v_pk_mul_f32 v[142:143], v[142:143], v[168:169] op_sel_hi:[1,0]\n"
    "v_cvt_pk_bf16_f32 v100, v136, v137\n"
    "v_cvt_pk_bf16_f32 v101, v138, v139\n"
    "v_cvt_pk_bf16_f32 v102, v140, v141\n"
    "v_cvt_pk_bf16_f32 v103, v142, v143\n"
    "buffer_store_dwordx4 v[100:103], v49, s[20:23], s4 offen offset:1024 sc1\n"
    "v_pk_mul_f32 v[144:145], v[144:145], v[176:177] op_sel_hi:[1,0]\n"
    "v_pk_mul_f32 v[146:147], v[146:147], v[176:177] op_sel_hi:[1,0]\n"
    "v_pk_mul_f32 v[148:149], v[148:149], v[176:177] op_sel_hi:[1,0]\n"
    "v_pk_mul_f32 v[150:151], v[150:151], v[176:177] op_sel_hi:[1,0]\n"
    "v_cvt_pk_bf16_f32 v104, v144, v145\n"
    "v_cvt_pk_bf16_f32 v105, v146, v147\n"
    "v_cvt_pk_bf16_f32 v106, v148, v149\n"
    "v_cvt_pk_bf16_f32 v107, v150, v151\n"
    "buffer_store_dwordx4 v[104:107], v49, s[20:23], s5 offen offset:0 sc1\n"
    "v_pk_mul_f32 v[152:153], v[152:153], v[184:185] op_sel_hi:[1,0]\n"
    "v_pk_mul_f32 v[154:155], v[154:155], v[184:185] op_sel_hi:[1,0]\n"
    "v_pk_mul_f32 v[156:157], v[156:157], v[184:185] op_sel_hi:[1,0]\n"
    "v_pk_mul_f32 v[158:159], v[158:159], v[184:185] op_sel_hi:[1,0]\n"
    "v_cvt_pk_bf16_f32 v108, v152, v153\n"
    "v_cvt_pk_bf16_f32 v109, v154, v155\n"
    "v_cvt_pk_bf16_f32 v110, v156, v157\n"
    "v_cvt_pk_bf16_f32 v111, v158, v159\n"
    "buffer_store_dwordx4 v[108:111], v49, s[20:23], s5 offen offset:1024 sc1\n"
    "s_and_b32 s72, s69, 1\n"
    "s_lshr_b32 s73, s69, 1\n"
    "s_and_b32 s76, s73, 1\n"
    "v_lshl_add_u32 v26, v1, 4, s77\n"
    "s_add_u32 m0, s77, 32768\n"
    "s_mov_b32 s4, 131072\n"
    "buffer_load_dwordx4 v14, s[28:31], s4 offen offset:0 lds\n"
    "buffer_load_dwordx4 v15, s[28:31], s4 offen offset:1024 lds\n"
    "buffer_load_dwordx4 v16, s[28:31], s4 offen offset:2048 lds\n"
    "v_add_u32 v27, 16384, v26\n"
    "buffer_load_dwordx4 v17, s[28:31], s4 offen offset:3072 lds\n"
    "v_and_b32 v6, 31, v1\n"
    "s_add_u32 m0, s77, 49152\n"
    "s_mov_b32 s4, 196608\n"
    "buffer_load_dwordx4 v14, s[28:31], s4 offen offset:0 lds\n"
    "v_lshrrev_b32 v7, 5, v1\n"
    "buffer_load_dwordx4 v15, s[28:31], s4 offen offset:1024 lds\n"
    "v_lshlrev_b32 v40, 4, v1\n"
    "s_lshl_b32 s7, s65, 7\n"
    "v_lshlrev_b32 v2, 14, v7\n"
    "buffer_load_dwordx4 v16, s[28:31], s4 offen offset:2048 lds\n"
    "buffer_load_dwordx4 v17, s[28:31], s4 offen offset:3072 lds\n"
    "v_lshl_add_u32 v2, v6, 2, v2\n"
    "v_add_u32 v136, s7, v2\n"
    "ds_read_b64_tr_b16 a[64:65], v53 offset:0\n"
    "v_add_u32 v137, 4096, v136\n"
    "ds_read_b64_tr_b16 a[66:67], v53 offset:2048\n"
    "v_add_u32 v138, 8192, v136\n"
    "ds_read_b64_tr_b16 a[68:69], v53 offset:4096\n"
    "v_add_u32 v139, 12288, v136\n"
    "v_add_u32 v140, 32768, v136\n"
    "ds_read_b64_tr_b16 a[70:71], v53 offset:6144\n"
    "v_add_u32 v141, 36864, v136\n"
    "ds_read_b64_tr_b16 a[72:73], v53 offset:8192\n"
    "v_add_u32 v142, 40960, v136\n"
    "ds_read_b64_tr_b16 a[74:75], v53 offset:10240\n"
    "v_add_u32 v143, 45056, v136\n"
    "v_add_u32 v144, 65536, v136\n"
    "ds_read_b64_tr_b16 a[76:77], v53 offset:12288\n"
    "v_add_u32 v145, 69632, v136\n"
    "ds_read_b64_tr_b16 a[78:79], v53 offset:14336\n"
    "v_add_u32 v146, 73728, v136\n"
    "v_add_u32 v147, 77824, v136\n"
    "ds_read_b64_tr_b16 a[80:81], v53 offset:16384\n"
    "v_add_u32 v148, 98304, v136\n"
    "ds_read_b64_tr_b16 a[82:83], v53 offset:18432\n"
    "v_add_u32 v149, 102400, v136\n"
    "ds_read_b64_tr_b16 a[84:85], v53 offset:20480\n"
    "v_add_u32 v150, 106496, v136\n"
    "v_add_u32 v151, 110592, v136\n"
    "s_lshl_b32 s34, s68, 1\n"
    "s_add_u32 s34, s34, s72\n"
    "s_lshl_b32 s34, s34, 18\n"
    "s_lshl_b32 s35, s73, 9\n"
    "s_add_u32 s35, s35, 0\n"
    "s_add_u32 s34, s34, s35\n"
    "s_sub_u32 s7, 262144, s35\n"
    "s_add_u32 s24, s62, s34\n"
    "s_addc_u32 s25, s63, 0\n"
    "s_and_b32 s25, s25, 0xffff\n"
    "s_mov_b32 s26, s7\n"
    "s_mov_b32 s27, 0x20000\n"
    "s_lshl_b32 s34, s68, 2\n"
    "s_add_u32 s34, s34, s72\n"
    "s_lshl_b32 s34, s34, 15\n"
    "s_add_u32 s34, s34, 1048576\n"
    "s_add_u32 s8, s46, s34\n"
    "s_addc_u32 s9, s47, 0\n"
    "s_and_b32 s9, s9, 0xffff\n"
    "s_mov_b32 s10, 98304\n"
    "s_mov_b32 s11, 0x20000\n"
    "s_lshl_b32 s74, s76, 16\n"
    "s_xor_b32 s78, s74, 65536\n"
    "s_lshl_b32 s18, s76, 17\n"
    "s_xor_b32 s19, s18, 131072\n"
    "s_lshl_b32 s34, s68, 10\n"
    "s_add_u32 s34, s34, 589824\n"
    "v_and_b32 v28, 7, v1\n"
    "ds_read_b64_tr_b16 a[86:87], v53 offset:22528\n"
    "ds_read_b64_tr_b16 a[88:89], v53 offset:24576\n"
    "ds_read_b64_tr_b16 a[90:91], v53 offset:26624\n"
    "ds_read_b64_tr_b16 a[92:93], v53 offset:28672\n"
    "ds_read_b64_tr_b16 a[94:95], v53 offset:30720\n"
    "ds_read_b64_tr_b16 a[96:97], v53 offset:32768\n"
    "ds_read_b64_tr_b16 a[98:99], v53 offset:34816\n"
    "ds_read_b64_tr_b16 a[100:101], v53 offset:36864\n"
    "ds_read_b64_tr_b16 a[102:103], v53 offset:38912\n"
    "ds_read_b64_tr_b16 a[104:105], v53 offset:40960\n"
    "ds_read_b64_tr_b16 a[106:107], v53 offset:43008\n"
    "ds_read_b64_tr_b16 a[108:109], v53 offset:45056\n"
    "ds_read_b64_tr_b16 a[110:111], v53 offset:47104\n"
    "ds_read_b64_tr_b16 a[112:113], v53 offset:49152\n"
    "ds_read_b64_tr_b16 a[114:115], v53 offset:51200\n"
    "ds_read_b64_tr_b16 a[116:117], v53 offset:53248\n"
    "ds_read_b64_tr_b16 a[118:119], v53 offset:55296\n"
    "ds_read_b64_tr_b16 a[120:121], v53 offset:57344\n"
    "ds_read_b64_tr_b16 a[122:123], v53 offset:59392\n"
    "ds_read_b64_tr_b16 a[124:125], v53 offset:61440\n"
    "ds_read_b64_tr_b16 a[126:127], v53 offset:63488\n"
    "v_lshl_add_u32 v28, v28, 7, s34\n"
    "s_waitcnt vmcnt(8)\n"
    "s_barrier\n"
    "buffer_store_dwordx2 v[50:51], off, s[20:23], s17 sc1\n"
    "s_waitcnt vmcnt(1)\n"
    "s_barrier\n"
    "ds_read_b64_tr_b16 a[0:1], v52 offset:0\n"
    "ds_read_b64_tr_b16 a[2:3], v52 offset:2048\n"
    "ds_read_b64_tr_b16 a[4:5], v52 offset:4096\n"
    "ds_read_b64_tr_b16 a[6:7], v52 offset:6144\n"
    "ds_read_b64_tr_b16 a[8:9], v52 offset:8192\n"
    "ds_read_b64_tr_b16 a[10:11], v52 offset:10240\n"
    "ds_read_b64_tr_b16 a[12:13], v52 offset:12288\n"
    "ds_read_b64_tr_b16 a[14:15], v52 offset:14336\n"
    "ds_read_b64_tr_b16 a[16:17], v52 offset:16384\n"
    "ds_read_b64_tr_b16 a[18:19], v52 offset:18432\n"
    "ds_read_b64_tr_b16 a[20:21], v52 offset:20480\n"
    "ds_read_b64_tr_b16 a[22:23], v52 offset:22528\n"
    "ds_read_b64_tr_b16 a[24:25], v52 offset:24576\n"
    "ds_read_b64_tr_b16 a[26:27], v52 offset:26624\n"
    "ds_read_b64_tr_b16 a[28:29], v52 offset:28672\n"
    "ds_read_b64_tr_b16 a[30:31], v52 offset:30720\n"
    "ds_read_b64_tr_b16 a[32:33], v52 offset:32768\n"
    "ds_read_b64_tr_b16 a[34:35], v52 offset:34816\n"
    "ds_read_b64_tr_b16 a[36:37], v52 offset:36864\n"
    "ds_read_b64_tr_b16 a[38:39], v52 offset:38912\n"
    "ds_read_b64_tr_b16 a[40:41], v52 offset:40960\n"
    "ds_read_b64_tr_b16 a[42:43], v52 offset:43008\n"
    "ds_read_b64_tr_b16 a[44:45], v52 offset:45056\n"
    "ds_read_b64_tr_b16 a[46:47], v52 offset:47104\n"
    "ds_read_b64_tr_b16 a[48:49], v52 offset:49152\n"
    "ds_read_b64_tr_b16 a[50:51], v52 offset:51200\n"
    "ds_read_b64_tr_b16 a[52:53], v52 offset:53248\n"
    "ds_read_b64_tr_b16 a[54:55], v52 offset:55296\n"
    "ds_read_b64_tr_b16 a[56:57], v52 offset:57344\n"
    "ds_read_b64_tr_b16 a[58:59], v52 offset:59392\n"
    "ds_read_b64_tr_b16 a[60:61], v52 offset:61440\n"
    "ds_read_b64_tr_b16 a[62:63], v52 offset:63488\n"
    "s_waitcnt lgkmcnt(0)\n"
    "s_cmp_lg_u32 s65, 1\n"
    "s_cbranch_scc1 Lpw_17%=\n"
    "s_mov_b32 s79, 65536\n"
    "Lpoll_18%=:\n"
    "buffer_load_dwordx2 v[8:9], v28, s[20:23], 0 offen sc1\n"
    "s_sub_u32 s79, s79, 1\n"
    "s_cbranch_scc1 Lpdone_19%=\n"
    "s_waitcnt vmcnt(0)\n"
    "v_cmp_ne_u64 vcc, s[80:81], v[8:9]\n"
    "s_nop 0\n"
    "s_cbranch_vccnz Lpoll_18%=\n"
    "Lpdone_19%=:\n"
    "s_waitcnt vmcnt(0)\n"
    "Lpw_17%=:\n"
    "s_barrier\n"
    "s_add_u32 m0, s77, 0\n"
    "s_xor_b32 s4, s74, 0\n"
    "buffer_load_dwordx4 v26, s[8:11], s4 offen offset:0 sc1 lds\n"
    "buffer_load_dwordx4 v26, s[8:11], s4 offen offset:1024 sc1 lds\n"
    "buffer_load_dwordx4 v26, s[8:11], s4 offen offset:2048 sc1 lds\n"
    "buffer_load_dwordx4 v26, s[8:11], s4 offen offset:3072 sc1 lds\n"
    "s_add_u32 m0, m0, 0x4000\n"
    "s_nop 0\n"
    "buffer_load_dwordx4 v27, s[8:11], s4 offen offset:0 sc1 lds\n"
    "buffer_load_dwordx4 v27, s[8:11], s4 offen offset:1024 sc1 lds\n"
    "buffer_load_dwordx4 v27, s[8:11], s4 offen offset:2048 sc1 lds\n"
    "buffer_load_dwordx4 v27, s[8:11], s4 offen offset:3072 sc1 lds\n"
    "s_add_u32 m0, s77, 32768\n"
    "s_xor_b32 s4, s78, 0\n"
    "buffer_load_dwordx4 v26, s[8:11], s4 offen offset:0 sc1 lds\n"
    "buffer_load_dwordx4 v26, s[8:11], s4 offen offset:1024 sc1 lds\n"
    "buffer_load_dwordx4 v26, s[8:11], s4 offen offset:2048 sc1 lds\n"
    "buffer_load_dwordx4 v26, s[8:11], s4 offen offset:3072 sc1 lds\n"
    "s_add_u32 m0, m0, 0x4000\n"
    "s_nop 0\n"
    "buffer_load_dwordx4 v27, s[8:11], s4 offen offset:0 sc1 lds\n"
    "buffer_load_dwordx4 v27, s[8:11], s4 offen offset:1024 sc1 lds\n"
    "buffer_load_dwordx4 v27, s[8:11], s4 offen offset:2048 sc1 lds\n"
    "buffer_load_dwordx4 v27, s[8:11], s4 offen offset:3072 sc1 lds\n"
    "s_or_b32 s4, s69, s65\n"
    "s_xor_b32 s5, s68, 0\n"
    "s_or_b32 s4, s4, s5\n"
    "s_cmp_lg_u32 s4, 0\n"
    "s_cbranch_scc1 Lnb_20%=\n"
    "v_mov_b32 v2, s80\n"
    "v_mov_b32 v3, s81\n"
    "s_movk_i32 s4, 0\n"
    "buffer_store_dwordx2 v[2:3], off, s[20:23], s4 sc1\n"
    "Lnb_20%=:\n"
    "s_waitcnt vmcnt(8)\n"
    "s_barrier\n"
    "ds_read_b128 v[96:99], v40 offset:0\n"
    "ds_read_b128 v[100:103], v40 offset:1024\n"
    "ds_read_b128 v[104:107], v40 offset:2048\n"
    "ds_read_b128 v[108:111], v40 offset:3072\n"
    "ds_read_b128 v[112:115], v40 offset:4096\n"
    "ds_read_b128 v[116:119], v40 offset:5120\n"
    "s_waitcnt lgkmcnt(4)\n"
    "v_mfma_f32_32x32x16_bf16 v[64:79], v[96:99], a[0:3], 0\n"
    "ds_read_b128 v[120:123], v40 offset:6144\n"
    "v_mfma_f32_32x32x16_bf16 v[64:79], v[100:103], a[4:7], v[64:79]\n"
    "ds_read_b128 v[124:127], v40 offset:7168\n"
    "s_waitcnt lgkmcnt(4)\n"
    "v_mfma_f32_32x32x16_bf16 v[64:79], v[104:107], a[8:11], v[64:79]\n"
    "ds_read_b128 v[96:99], v40 offset:8192\n"
    "v_mfma_f32_32x32x16_bf16 v[64:79], v[108:111], a[12:15], v[64:79]\n"
    "ds_read_b128 v[100:103], v40 offset:9216\n"
    "s_waitcnt lgkmcnt(4)\n"
    "v_mfma_f32_32x32x16_bf16 v[64:79], v[112:115], a[16:19], v[64:79]\n"
    "ds_read_b128 v[104:107], v40 offset:10240\n"
    "v_mfma_f32_32x32x16_bf16 v[64:79], v[116:119], a[20:23], v[64:79]\n"
    "ds_read_b128 v[108:111], v40 offset:11264\n"
    "s_waitcnt lgkmcnt(4)\n"
    "v_mfma_f32_32x32x16_bf16 v[64:79], v[120:123], a[24:27], v[64:79]\n"
    "ds_read_b128 v[112:115], v40 offset:12288\n"
    "v_mfma_f32_32x32x16_bf16 v[64:79], v[124:127], a[28:31], v[64:79]\n"
    "ds_read_b128 v[116:119], v40 offset:13312\n"
    "s_waitcnt lgkmcnt(4)\n"
    "v_mfma_f32_32x32x16_bf16 v[64:79], v[96:99], a[32:35], v[64:79]\n"
    "ds_read_b128 v[120:123], v40 offset:14336\n"
    "v_mfma_f32_32x32x16_bf16 v[64:79], v[100:103], a[36:39], v[64:79]\n"
    "ds_read_b128 v[124:127], v40 offset:15360\n"
    "s_waitcnt lgkmcnt(4)\n"
    "v_mfma_f32_32x32x16_bf16 v[64:79], v[104:107], a[40:43], v[64:79]\n"
    "ds_read_b128 v[96:99], v40 offset:16384\n"
    "v_mfma_f32_32x32x16_bf16 v[64:79], v[108:111], a[44:47], v[64:79]\n"
    "ds_read_b128 v[100:103], v40 offset:17408\n"
    "s_waitcnt lgkmcnt(4)\n"
    "v_mfma_f32_32x32x16_bf16 v[64:79], v[112:115], a[48:51], v[64:79]\n"
    "ds_read_b128 v[104:107], v40 offset:18432\n"
    "v_mfma_f32_32x32x16_bf16 v[64:79], v[116:119], a[52:55], v[64:79]\n"
    "ds_read_b128 v[108:111], v40 offset:19456\n"
    "s_waitcnt lgkmcnt(4)\n"
    "v_mfma_f32_32x32x16_bf16 v[64:79], v[120:123], a[56:59], v[64:79]\n"
    "ds_read_b128 v[112:115], v40 offset:20480\n"
    "v_mfma_f32_32x32x16_bf16 v[64:79], v[124:127], a[60:63], v[64:79]\n"
    "ds_read_b128 v[116:119], v40 offset:21504\n"
    "s_waitcnt lgkmcnt(4)\n"
    "v_mfma_f32_32x32x16_bf16 v[64:79], v[96:99], a[64:67], v[64:79]\n"
    "ds_read_b128 v[120:123], v40 offset:22528\n"
    "v_mfma_f32_32x32x16_bf16 v[64:79], v[100:103], a[68:71], v[64:79]\n"
    "ds_read_b128 v[124:127], v40 offset:23552\n"
    "s_waitcnt lgkmcnt(4)\n"
    "v_mfma_f32_32x32x16_bf16 v[64:79], v[104:107], a[72:75], v[64:79]\n"
    "ds_read_b128 v[96:99], v40 offset:24576\n"
    "v_mfma_f32_32x32x16_bf16 v[64:79], v[108:111], a[76:79], v[64:79]\n"
    "ds_read_b128 v[100:103], v40 offset:25600\n"
    "s_waitcnt lgkmcnt(4)\n"
    "v_mfma_f32_32x32x16_bf16 v[64:79], v[112:115], a[80:83], v[64:79]\n"
    "ds_read_b128 v[104:107], v40 offset:26624\n"
    "v_mfma_f32_32x32x16_bf16 v[64:79], v[116:119], a[84:87], v[64:79]\n"
    "ds_read_b128 v[108:111], v40 offset:27648\n"
    "s_waitcnt lgkmcnt(4)\n"
    "v_mfma_f32_32x32x16_bf16 v[64:79], v[120:123], a[88:91], v[64:79]\n"
    "ds_read_b128 v[112:115], v40 offset:28672\n"
    "v_mfma_f32_32x32x16_bf16 v[64:79], v[124:127], a[92:95], v[64:79]\n"
    "ds_read_b128 v[116:119], v40 offset:29696\n"
    "s_waitcnt lgkmcnt(4)\n"
    "v_mfma_f32_32x32x16_bf16 v[64:79], v[96:99], a[96:99], v[64:79]\n"
    "ds_read_b128 v[120:123], v40 offset:30720\n"
    "v_mfma_f32_32x32x16_bf16 v[64:79], v[100:103], a[100:103], v[64:79]\n"
    "ds_read_b128 v[124:127], v40 offset:31744\n"
    "s_waitcnt vmcnt(0)\n"
    "s_barrier\n"
    "s_waitcnt lgkmcnt(4)\n"
    "v_mfma_f32_32x32x16_bf16 v[64:79], v[104:107], a[104:107], v[64:79]\n"
    "ds_read_b128 v[96:99], v40 offset:32768\n"
    "v_mfma_f32_32x32x16_bf16 v[64:79], v[108:111], a[108:111], v[64:79]\n"
    "ds_read_b128 v[100:103], v40 offset:33792\n"
    "s_waitcnt lgkmcnt(4)\n"
    "v_mfma_f32_32x32x16_bf16 v[64:79], v[112:115], a[112:115], v[64:79]\n"
    "ds_read_b128 v[104:107], v40 offset:34816\n"
    "v_mfma_f32_32x32x16_bf16 v[64:79], v[116:119], a[116:119], v[64:79]\n"
    "ds_read_b128 v[108:111], v40 offset:35840\n"
    "s_waitcnt lgkmcnt(4)\n"
    "v_mfma_f32_32x32x16_bf16 v[64:79], v[120:123], a[120:123], v[64:79]\n"
    "ds_read_b128 v[112:115], v40 offset:36864\n"
    "v_mfma_f32_32x32x16_bf16 v[64:79], v[124:127], a[124:127], v[64:79]\n"
    "ds_read_b128 v[116:119], v40 offset:37888\n"
    "s_waitcnt lgkmcnt(4)\n"
    "v_mfma_f32_32x32x16_bf16 v[80:95], v[96:99], a[0:3], 0\n"
    "ds_read_b128 v[120:123], v40 offset:38912\n"
    "v_mfma_f32_32x32x16_bf16 v[80:95], v[100:103], a[4:7], v[80:95]\n"
    "ds_read_b128 v[124:127], v40 offset:39936\n"
    "s_waitcnt lgkmcnt(4)\n"
    "v_mfma_f32_32x32x16_bf16 v[80:95], v[104:107], a[8:11], v[80:95]\n"
    "ds_read_b128 v[96:99], v40 offset:40960\n"
    "s_nop 2\n"
    "buffer_store_dword v64, v136, s[24:27], s18 offen sc1\n"
    "v_mfma_f32_32x32x16_bf16 v[80:95], v[108:111], a[12:15], v[80:95]\n"
    "ds_read_b128 v[100:103], v40 offset:41984\n"
    "buffer_store_dword v65, v137, s[24:27], s18 offen sc1\n"
    "s_waitcnt lgkmcnt(4)\n"
    "v_mfma_f32_32x32x16_bf16 v[80:95], v[112:115], a[16:19], v[80:95]\n"
    "ds_read_b128 v[104:107], v40 offset:43008\n"
    "buffer_store_dword v66, v138, s[24:27], s18 offen sc1\n"
    "v_mfma_f32_32x32x16_bf16 v[80:95], v[116:119], a[20:23], v[80:95]\n"
    "ds_read_b128 v[108:111], v40 offset:44032\n"
    "buffer_store_dword v67, v139, s[24:27], s18 offen sc1\n"
    "s_waitcnt lgkmcnt(4)\n"
    "v_mfma_f32_32x32x16_bf16 v[80:95], v[120:123], a[24:27], v[80:95]\n"
    "ds_read_b128 v[112:115], v40 offset:45056\n"
    "buffer_store_dword v68, v140, s[24:27], s18 offen sc1\n"
    "v_mfma_f32_32x32x16_bf16 v[80:95], v[124:127], a[28:31], v[80:95]\n"
    "ds_read_b128 v[116:119], v40 offset:46080\n"
    "buffer_store_dword v69, v141, s[24:27], s18 offen sc1\n"
    "s_waitcnt lgkmcnt(4)\n"
    "v_mfma_f32_32x32x16_bf16 v[80:95], v[96:99], a[32:35], v[80:95]\n"
    "ds_read_b128 v[120:123], v40 offset:47104\n"
    "buffer_store_dword v70, v142, s[24:27], s18 offen sc1\n"
    "v_mfma_f32_32x32x16_bf16 v[80:95], v[100:103], a[36:39], v[80:95]\n"
    "ds_read_b128 v[124:127], v40 offset:48128\n"
    "buffer_store_dword v71, v143, s[24:27], s18 offen sc1\n"
    "s_waitcnt lgkmcnt(4)\n"
    "v_mfma_f32_32x32x16_bf16 v[80:95], v[104:107], a[40:43], v[80:95]\n"
    "ds_read_b128 v[96:99], v40 offset:49152\n"
    "buffer_store_dword v72, v144, s[24:27], s18 offen sc1\n"
    "v_mfma_f32_32x32x16_bf16 v[80:95], v[108:111], a[44:47], v[80:95]\n"
    "ds_read_b128 v[100:103], v40 offset:50176\n"
    "buffer_store_dword v73, v145, s[24:27], s18 offen sc1\n"
    "s_waitcnt lgkmcnt(4)\n"
    "v_mfma_f32_32x32x16_bf16 v[80:95], v[112:115], a[48:51], v[80:95]\n"
    "ds_read_b128 v[104:107], v40 offset:51200\n"
    "buffer_store_dword v74, v146, s[24:27], s18 offen sc1\n"
    "v_mfma_f32_32x32x16_bf16 v[80:95], v[116:119], a[52:55], v[80:95]\n"
    "ds_read_b128 v[108:111], v40 offset:52224\n"
    "buffer_store_dword v75, v147, s[24:27], s18 offen sc1\n"
    "s_waitcnt lgkmcnt(4)\n"
    "v_mfma_f32_32x32x16_bf16 v[80:95], v[120:123], a[56:59], v[80:95]\n"
    "ds_read_b128 v[112:115], v40 offset:53248\n"
    "buffer_store_dword v76, v148, s[24:27], s18 offen sc1\n"
    "v_mfma_f32_32x32x16_bf16 v[80:95], v[124:127], a[60:63], v[80:95]\n"
    "ds_read_b128 v[116:119], v40 offset:54272\n"
    "buffer_store_dword v77, v149, s[24:27], s18 offen sc1\n"
    "s_waitcnt lgkmcnt(4)\n"
    "v_mfma_f32_32x32x16_bf16 v[80:95], v[96:99], a[64:67], v[80:95]\n"
    "ds_read_b128 v[120:123], v40 offset:55296\n"
    "buffer_store_dword v78, v150, s[24:27], s18 offen sc1\n"
    "v_mfma_f32_32x32x16_bf16 v[80:95], v[100:103], a[68:71], v[80:95]\n"
    "ds_read_b128 v[124:127], v40 offset:56320\n"
    "buffer_store_dword v79, v151, s[24:27], s18 offen sc1\n"
    "s_waitcnt lgkmcnt(4)\n"
    "v_mfma_f32_32x32x16_bf16 v[80:95], v[104:107], a[72:75], v[80:95]\n"
    "ds_read_b128 v[96:99], v40 offset:57344\n"
    "v_mfma_f32_32x32x16_bf16 v[80:95], v[108:111], a[76:79], v[80:95]\n"
    "ds_read_b128 v[100:103], v40 offset:58368\n"
    "s_waitcnt lgkmcnt(4)\n"
    "v_mfma_f32_32x32x16_bf16 v[80:95], v[112:115], a[80:83], v[80:95]\n"
    "ds_read_b128 v[104:107], v40 offset:59392\n"
    "v_mfma_f32_32x32x16_bf16 v[80:95], v[116:119], a[84:87], v[80:95]\n"
    "ds_read_b128 v[108:111], v40 offset:60416\n"
    "s_waitcnt lgkmcnt(4)\n"
    "v_mfma_f32_32x32x16_bf16 v[80:95], v[120:123], a[88:91], v[80:95]\n"
    "ds_read_b128 v[112:115], v40 offset:61440\n"
    "v_mfma_f32_32x32x16_bf16 v[80:95], v[124:127], a[92:95], v[80:95]\n"
    "ds_read_b128 v[116:119], v40 offset:62464\n"
    "s_waitcnt lgkmcnt(4)\n"
    "v_mfma_f32_32x32x16_bf16 v[80:95], v[96:99], a[96:99], v[80:95]\n"
    "ds_read_b128 v[120:123], v40 offset:63488\n"
    "v_mfma_f32_32x32x16_bf16 v[80:95], v[100:103], a[100:103], v[80:95]\n"
    "ds_read_b128 v[124:127], v40 offset:64512\n"
    "s_waitcnt lgkmcnt(4)\n"
    "v_mfma_f32_32x32x16_bf16 v[80:95], v[104:107], a[104:107], v[80:95]\n"
    "v_mfma_f32_32x32x16_bf16 v[80:95], v[108:111], a[108:111], v[80:95]\n"
    "s_waitcnt lgkmcnt(2)\n"
    "v_mfma_f32_32x32x16_bf16 v[80:95], v[112:115], a[112:115], v[80:95]\n"
    "v_mfma_f32_32x32x16_bf16 v[80:95], v[116:119], a[116:119], v[80:95]\n"
    "s_waitcnt lgkmcnt(0)\n"
    "v_mfma_f32_32x32x16_bf16 v[80:95], v[120:123], a[120:123], v[80:95]\n"
    "v_mfma_f32_32x32x16_bf16 v[80:95], v[124:127], a[124:127], v[80:95]\n"
    "s_nop 11\n"
    "buffer_store_dword v80, v136, s[24:27], s19 offen sc1\n"
    "buffer_store_dword v81, v137, s[24:27], s19 offen sc1\n"
    "buffer_store_dword v82, v138, s[24:27], s19 offen sc1\n"
    "buffer_store_dword v83, v139, s[24:27], s19 offen sc1\n"
    "buffer_store_dword v84, v140, s[24:27], s19 offen sc1\n"
    "buffer_store_dword v85, v141, s[24:27], s19 offen sc1\n"
    "buffer_store_dword v86, v142, s[24:27], s19 offen sc1\n"
    "buffer_store_dword v87, v143, s[24:27], s19 offen sc1\n"
    "buffer_store_dword v88, v144, s[24:27], s19 offen sc1\n"
    "buffer_store_dword v89, v145, s[24:27], s19 offen sc1\n"
    "buffer_store_dword v90, v146, s[24:27], s19 offen sc1\n"
    "buffer_store_dword v91, v147, s[24:27], s19 offen sc1\n"
    "buffer_store_dword v92, v148, s[24:27], s19 offen sc1\n"
    "buffer_store_dword v93, v149, s[24:27], s19 offen sc1\n"
    "buffer_store_dword v94, v150, s[24:27], s19 offen sc1\n"
    "buffer_store_dword v95, v151, s[24:27], s19 offen sc1\n"
    "s_endpgm\n"
    "Lend%=:\n"
    :: "s"(__builtin_amdgcn_kernarg_segment_ptr()), "s"(blockIdx.x), "v"(threadIdx.x)
    : "v1", "v2", "v3", "v4", "v5", "v6", "v7", "v8", "v9", "v10", "v11", "v12", "v13", "v14", "v15", "v16",
      "v17", "v18", "v19", "v20", "v21", "v22", "v23", "v24", "v25", "v26", "v27", "v28", "v29", "v30", "v31", "v32",
      "v33", "v34", "v35", "v36", "v37", "v38", "v39", "v40", "v41", "v42", "v43", "v44", "v45", "v46", "v47", "v48",
      "v49", "v50", "v51", "v52", "v53", "v54", "v55", "v56", "v57", "v58", "v59", "v60", "v61", "v62", "v63", "v64",
      "v65", "v66", "v67", "v68", "v69", "v70", "v71", "v72", "v73", "v74", "v75", "v76", "v77", "v78", "v79", "v80",
      "v81", "v82", "v83", "v84", "v85", "v86", "v87", "v88", "v89", "v90", "v91", "v92", "v93", "v94", "v95", "v96",
      "v97", "v98", "v99", "v100", "v101", "v102", "v103", "v104", "v105", "v106", "v107", "v108", "v109", "v110", "v111", "v112",
      "v113", "v114", "v115", "v116", "v117", "v118", "v119", "v120", "v121", "v122", "v123", "v124", "v125", "v126", "v127", "v128",
      "v129", "v130", "v131", "v132", "v133", "v134", "v135", "v136", "v137", "v138", "v139", "v140", "v141", "v142", "v143", "v144",
      "v145", "v146", "v147", "v148", "v149", "v150", "v151", "v152", "v153", "v154", "v155", "v156", "v157", "v158", "v159", "v160",
      "v161", "v162", "v163", "v164", "v165", "v166", "v167", "v168", "v169", "v170", "v171", "v172", "v173", "v174", "v175", "v176",
      "v177", "v178", "v179", "v180", "v181", "v182", "v183", "v184", "v185", "v186", "v187", "v188", "v189", "v190", "v191", "v192",
      "v193", "v194", "v195", "v196", "v197", "v198", "v199", "v200", "v201", "v202", "v203", "v204", "v205", "v206", "v207", "v208",
      "v209", "v210", "v211", "v212", "v213", "v214", "v215", "v216", "v217", "v218", "v219", "v220", "v221", "v222", "v223", "v224",
      "v225", "v226", "v227", "v228", "v229", "v230", "v231", "v232", "v233", "v234", "v235", "v236", "v237", "v238", "v239", "v240",
      "v241", "v242", "v243", "v244", "v245", "v246", "v247", "v248", "v249", "v250", "v251", "v252", "v253", "v254", "v255", "a0",
      "a1", "a2", "a3", "a4", "a5", "a6", "a7", "a8", "a9", "a10", "a11", "a12", "a13", "a14", "a15", "a16",
      "a17", "a18", "a19", "a20", "a21", "a22", "a23", "a24", "a25", "a26", "a27", "a28", "a29", "a30", "a31", "a32",
      "a33", "a34", "a35", "a36", "a37", "a38", "a39", "a40", "a41", "a42", "a43", "a44", "a45", "a46", "a47", "a48",
      "a49", "a50", "a51", "a52", "a53", "a54", "a55", "a56", "a57", "a58", "a59", "a60", "a61", "a62", "a63", "a64",
      "a65", "a66", "a67", "a68", "a69", "a70", "a71", "a72", "a73", "a74", "a75", "a76", "a77", "a78", "a79", "a80",
      "a81", "a82", "a83", "a84", "a85", "a86", "a87", "a88", "a89", "a90", "a91", "a92", "a93", "a94", "a95", "a96",
      "a97", "a98", "a99", "a100", "a101", "a102", "a103", "a104", "a105", "a106", "a107", "a108", "a109", "a110", "a111", "a112",
      "a113", "a114", "a115", "a116", "a117", "a118", "a119", "a120", "a121", "a122", "a123", "a124", "a125", "a126", "a127", "a128",
      "a129", "a130", "a131", "a132", "a133", "a134", "a135", "a136", "a137", "a138", "a139", "a140", "a141", "a142", "a143", "a144",
      "a145", "a146", "a147", "a148", "a149", "a150", "a151", "a152", "a153", "a154", "a155", "a156", "a157", "a158", "a159", "a160",
      "a161", "a162", "a163", "a164", "a165", "a166", "a167", "a168", "a169", "a170", "a171", "a172", "a173", "a174", "a175", "a176",
      "a177", "a178", "a179", "a180", "a181", "a182", "a183", "a184", "a185", "a186", "a187", "a188", "a189", "a190", "a191", "a192",
      "a193", "a194", "a195", "a196", "a197", "a198", "a199", "a200", "a201", "a202", "a203", "a204", "a205", "a206", "a207", "a208",
      "a209", "a210", "a211", "a212", "a213", "a214", "a215", "a216", "a217", "a218", "a219", "a220", "a221", "a222", "a223", "a224",
      "a225", "a226", "a227", "a228", "a229", "a230", "a231", "a232", "a233", "a234", "a235", "a236", "a237", "a238", "a239", "a240",
      "a241", "a242", "a243", "a244", "a245", "a246", "a247", "a248", "a249", "a250", "a251", "a252", "a253", "a254", "a255", "s4",
      "s5", "s6", "s7", "s8", "s9", "s10", "s11", "s12", "s13", "s14", "s15", "s16", "s17", "s18", "s19", "s20",
      "s21", "s22", "s23", "s24", "s25", "s26", "s27", "s28", "s29", "s30", "s31", "vcc", "scc", "memory", "s34", "s35", "s36", "s37", "s38", "s39", "s40", "s41", "s42", "s43", "s44", "s45", "s46", "s47", "s48", "s49", "s50", "s51", "s52", "s53", "s54", "s55", "s56", "s57", "s58", "s59", "s60", "s61", "s62", "s63", "s64", "s65", "s66", "s67", "s68", "s69", "s70", "s71", "s72", "s73", "s74", "s75", "s76", "s77", "s78", "s79", "s80", "s81", "s82", "s83", "s84", "s85", "s86", "s87", "s88", "s89", "s90", "s91", "s92", "s93", "s94", "s95", "s96", "s97", "s98", "s99");
#endif
}

extern "C" void kernel_launch(void* const* d_in, const int* in_sizes, int n_in, void* d_out, int out_size,
                              void* d_ws, size_t ws_size, hipStream_t stream) {
  (void)hipFuncSetAttribute((const void*)fused_kernel, hipFuncAttributeMaxDynamicSharedMemorySize, 163840);
  fused_kernel<<<256, 256, 163840, stream>>>(d_in[0], d_in[1], d_in[3], d_in[6], d_in[7], d_ws, d_in[4], d_in[5], d_in[10], d_in[11], d_in[2], d_in[8], d_in[9], d_out);
}
```
